```python
import torch

def _softmax(x, axis=-1):
    return torch.softmax(x, dim=axis)
def _gelu(x):
    return torch.nn.functional.gelu(x, approximate="tanh")
def _silu(x):
    return torch.nn.functional.silu(x)
def _sigmoid(x):
    return torch.sigmoid(x)
def _layernorm(x, axis=-1, eps=1e-5):
    m = x.mean(dim=axis, keepdim=True)
    v = x.var(dim=axis, keepdim=True, unbiased=False)
    return (x - m) / torch.sqrt(v + eps)
def _scatter_add(tgt, idx, src, axis=0):
    return tgt.clone().index_add_(axis, idx, src)
def _segment_sum(src, seg, n):
    out = torch.zeros((n,) + src.shape[1:], dtype=src.dtype)
    return out.index_add_(0, seg, src)
def _pad(x, pw):
    flat = tuple(v for pair in reversed(pw) for v in pair)
    return torch.nn.functional.pad(x, flat)

kernel_name = 'concat_heads__linear_kernel_hedgehog__ssm_conv_s5__prompt'

def setup_inputs():

    _w = (lambda a: (lambda t: t.to(torch.bfloat16) if t.is_floating_point() else t)(torch.as_tensor(a)))
    def _A_setup():
        B, S, D, H = 1, 2048, 640, 32
        Dh = D // H
        return {
            "x": _w((lambda *s: torch.randn(*s, dtype=torch.bfloat16) * 0.4)(B, S, D)),
            "wqkv": _w((lambda *s: torch.randn(*s, dtype=torch.bfloat16) * 0.4)(D, 3 * D)),
            "w_phi": _w((lambda *s: torch.randn(*s, dtype=torch.bfloat16) * 0.4)(H, Dh, Dh) * (Dh ** -0.5)),
            "wo": _w((lambda *s: torch.randn(*s, dtype=torch.bfloat16) * 0.05)(D, D)),
        }
    _A_in = _A_setup()

    def _B_setup():
        B, S, D = 1, 2048, 640
        P = D
        Lambda_re = -0.5 * (lambda _x: torch.exp(torch.as_tensor(_x)))((lambda *s: torch.randn(*s, dtype=torch.bfloat16) * 0.8)(P))
        Lambda_im = (lambda *s: torch.randn(*s, dtype=torch.bfloat16) * 0.8)(P) * 1.0
        B_re = (lambda *s: torch.randn(*s, dtype=torch.bfloat16) * 0.8)(P, D) * (D ** -0.5)
        B_im = (lambda *s: torch.randn(*s, dtype=torch.bfloat16) * 0.8)(P, D) * (D ** -0.5)
        C_re = (lambda *s: torch.randn(*s, dtype=torch.bfloat16) * 0.8)(D, P) * (P ** -0.5)
        C_im = (lambda *s: torch.randn(*s, dtype=torch.bfloat16) * 0.8)(D, P) * (P ** -0.5)
        log_dt = (lambda *s: torch.randn(*s, dtype=torch.bfloat16) * 0.8)(P) * 0.3 - 2.0
        Dskip = (lambda *s: torch.randn(*s, dtype=torch.bfloat16) * 0.8)(D)
        return {
            "x": _w((lambda *s: torch.randn(*s, dtype=torch.bfloat16) * 0.05)(B, S, D)),
            "Lambda_re": _w(Lambda_re),
            "Lambda_im": _w(Lambda_im),
            "B_re": _w(B_re),
            "B_im": _w(B_im),
            "C_re": _w(C_re),
            "C_im": _w(C_im),
            "log_dt": _w(log_dt),
            "Dskip": _w(Dskip),
        }
    _B_in = _B_setup()

    return {
        "A_x": _A_in["x"],
        "A_wqkv": _A_in["wqkv"],
        "A_w_phi": _A_in["w_phi"],
        "A_wo": _A_in["wo"],
        "B_x": _B_in["x"],
        "B_Lambda_re": _B_in["Lambda_re"],
        "B_Lambda_im": _B_in["Lambda_im"],
        "B_B_re": _B_in["B_re"],
        "B_B_im": _B_in["B_im"],
        "B_C_re": _B_in["C_re"],
        "B_C_im": _B_in["C_im"],
        "B_log_dt": _B_in["log_dt"],
        "B_Dskip": _B_in["Dskip"],
    }

def reference(A_x, A_wqkv, A_w_phi, A_wo, B_x, B_Lambda_re, B_Lambda_im, B_B_re, B_B_im, B_C_re, B_C_im, B_log_dt, B_Dskip):
    def _A_ref(x, wqkv, w_phi, wo):
        B, S, D = x.shape
        H = 32; Dh = D // H
        qkv = (lambda x, *d: x.permute(*d))((x @ wqkv).reshape(B, S, 3, H, Dh), 2, 0, 3, 1, 4)
        q, k, v = qkv[0], qkv[1], qkv[2]
        pq = torch.einsum("bhsd,hdf->bhsf", q, w_phi)
        pk = torch.einsum("bhsd,hdf->bhsf", k, w_phi)
        phi_q = torch.concatenate([_softmax(pq, axis=-1), _softmax(-pq, axis=-1)], axis=-1)
        phi_k = torch.concatenate([_softmax(pk, axis=-1), _softmax(-pk, axis=-1)], axis=-1)
        kv = torch.einsum("bhsf,bhsd->bhfd", phi_k, v)
        z  = torch.einsum("bhsf->bhf", phi_k)
        num = torch.einsum("bhqf,bhfd->bhqd", phi_q, kv)
        den = torch.einsum("bhqf,bhf->bhq", phi_q, z)[..., None] + 1e-6
        o = num / den
        return (lambda x, *d: x.permute(*d))(o, 0, 2, 1, 3).reshape(B, S, D) @ wo

    def _B_ref(x, Lambda_re, Lambda_im, B_re, B_im, C_re, C_im, log_dt, Dskip):
        Bsz, L, H = x.shape
        Lam = Lambda_re + 1j * Lambda_im
        Bmat = B_re + 1j * B_im
        Cmat = C_re + 1j * C_im
        dt = torch.exp(log_dt)
        Lam_bar = torch.exp(dt * Lam)
        B_bar = ((Lam_bar - 1.0) / Lam)[:, None] * Bmat
        Bu = torch.einsum("bld,pd->blp", x + 0j, B_bar)
        h = torch.zeros_like(Bu[:, 0, :])
        hs = []
        for t in range(L):
            h = Lam_bar[None, :] * h + Bu[:, t, :]
            hs.append(h)
        hseq = torch.stack(hs, 1)
        y = 2.0 * torch.einsum("blp,dp->bld", hseq, Cmat).real + x * Dskip[None, None, :]
        return y

    a = _A_ref(A_x, A_wqkv, A_w_phi, A_wo)
    b = _B_ref(B_x, B_Lambda_re, B_Lambda_im, B_B_re, B_B_im, B_C_re, B_C_im, B_log_dt, B_Dskip)
    return torch.concatenate([a, b], axis=-1)
```

```cpp
#include <hip/hip_runtime.h>
#include <hip/hip_bf16.h>
typedef unsigned short u16;
__device__ __forceinline__ float bf2f(u16 x){ return __uint_as_float(((unsigned)x)<<16); }
__device__ __forceinline__ u16 f2bf(float x){ unsigned u=__float_as_uint(x); u += 0x7fffu + ((u>>16)&1u); return (u16)(u>>16); }
__device__ __forceinline__ float rbf(float x){ return bf2f(f2bf(x)); }
constexpr int S=2048, D=640, H=32, DH=20;

__global__ void k_qkv(const u16* x, const u16* w, u16* qkv){
  int n = blockIdx.x*blockDim.x+threadIdx.x; int s = blockIdx.y;
  if(n>=1920) return;
  float acc=0; for(int k=0;k<D;k++) acc += bf2f(x[s*D+k])*bf2f(w[k*1920+n]);
  qkv[s*1920+n]=f2bf(acc);
}
__global__ void k_phi(const u16* qkv, const u16* wphi, u16* phiq, u16* phik){
  int idx = blockIdx.x*blockDim.x+threadIdx.x;
  if(idx>=S*64) return;
  int s=idx/64, wh=(idx/32)&1, h=idx&31;
  float p[DH];
  for(int f=0;f<DH;f++){ float a=0; for(int d=0;d<DH;d++) a+=bf2f(qkv[s*1920+wh*640+h*20+d])*bf2f(wphi[h*400+d*20+f]); p[f]=rbf(a);}
  float mx=-1e30f, mn=1e30f; for(int f=0;f<DH;f++){mx=fmaxf(mx,p[f]); mn=fminf(mn,p[f]);}
  float s1=0,s2=0; for(int f=0;f<DH;f++){ s1+=expf(p[f]-mx); s2+=expf(mn-p[f]); }
  u16* o = (wh?phik:phiq) + (s*32+h)*40;
  for(int f=0;f<DH;f++){ o[f]=f2bf(expf(p[f]-mx)/s1); o[20+f]=f2bf(expf(mn-p[f])/s2);}
}
__global__ void k_kv(const u16* qkv, const u16* phik, float* kv){
  int idx = blockIdx.x*blockDim.x+threadIdx.x; if(idx>=32*40*21) return;
  int h=idx/840, f=(idx%840)/21, d=idx%21;
  float a=0; for(int s=0;s<S;s++){ float v = d<20? bf2f(qkv[s*1920+1280+h*20+d]) : 1.f; a+=bf2f(phik[(s*32+h)*40+f])*v; }
  kv[idx]=rbf(a);
}
__global__ void k_o(const u16* phiq, const float* kv, u16* o){
  int idx = blockIdx.x*blockDim.x+threadIdx.x; if(idx>=S*640) return;
  int s=idx/640, h=(idx%640)/20, d=idx%20;
  float num=0, den=0; for(int f=0;f<40;f++){ float q=bf2f(phiq[(s*32+h)*40+f]); num+=q*kv[h*840+f*21+d]; den+=q*kv[h*840+f*21+20]; }
  num=rbf(num); den=rbf(rbf(den)+1e-6f);
  o[idx]=f2bf(num/den);
}
__global__ void k_a(const u16* o, const u16* wo, float* out){
  int n = blockIdx.x*blockDim.x+threadIdx.x; int s = blockIdx.y; if(n>=640) return;
  float acc=0; for(int k=0;k<D;k++) acc+=bf2f(o[s*D+k])*bf2f(wo[k*D+n]);
  out[s*1280+n]=rbf(acc);
}
__global__ void k_bu(const u16* x, const u16* Bre, const u16* Bim, float* bu){
  int p = blockIdx.x*blockDim.x+threadIdx.x; int s=blockIdx.y; if(p>=640) return;
  float ar=0, ai=0; for(int k=0;k<D;k++){ float xv=bf2f(x[s*D+k]); ar+=xv*bf2f(Bre[p*D+k]); ai+=xv*bf2f(Bim[p*D+k]); }
  bu[(s*640+p)*2]=ar; bu[(s*640+p)*2+1]=ai;
}
__global__ void k_scan(const u16* lre, const u16* lim, const u16* ldt, float* bu){
  int p = blockIdx.x*blockDim.x+threadIdx.x; if(p>=640) return;
  float dt=rbf(expf(bf2f(ldt[p]))); float lr=bf2f(lre[p]), li=bf2f(lim[p]);
  float e=expf(dt*lr); float br=e*cosf(dt*li), bi=e*sinf(dt*li);
  float nr=br-1.f, ni=bi; float dd=lr*lr+li*li; float cr=(nr*lr+ni*li)/dd, ci=(ni*lr-nr*li)/dd;
  float hr=0, hi=0;
  for(int s=0;s<S;s++){ float ur=bu[(s*640+p)*2], ui=bu[(s*640+p)*2+1]; float xr=cr*ur-ci*ui, xi=cr*ui+ci*ur;
    float t=br*hr-bi*hi+xr; hi=br*hi+bi*hr+xi; hr=t; bu[(s*640+p)*2]=hr; bu[(s*640+p)*2+1]=hi; }
}
__global__ void k_y(const float* hs, const u16* Cre, const u16* Cim, const u16* x, const u16* dsk, float* out){
  int d = blockIdx.x*blockDim.x+threadIdx.x; int s=blockIdx.y; if(d>=640) return;
  float acc=0; for(int p=0;p<640;p++) acc += rbf(hs[(s*640+p)*2])*bf2f(Cre[d*640+p]) - rbf(hs[(s*640+p)*2+1])*bf2f(Cim[d*640+p]);
  out[s*1280+640+d]=2.f*acc + rbf(bf2f(x[s*D+d])*bf2f(dsk[d]));
}
extern "C" void kernel_launch(void* const* d_in, const int* in_sizes, int n_in, void* d_out, int out_size, void* d_ws, size_t ws_size, hipStream_t stream){
  const u16* Ax=(const u16*)d_in[0]; const u16* wqkv=(const u16*)d_in[1]; const u16* wphi=(const u16*)d_in[2]; const u16* wo=(const u16*)d_in[3];
  const u16* Bx=(const u16*)d_in[4]; const u16* lre=(const u16*)d_in[5]; const u16* lim=(const u16*)d_in[6];
  const u16* Bre=(const u16*)d_in[7]; const u16* Bim=(const u16*)d_in[8]; const u16* Cre=(const u16*)d_in[9]; const u16* Cim=(const u16*)d_in[10];
  const u16* ldt=(const u16*)d_in[11]; const u16* dsk=(const u16*)d_in[12];
  char* ws=(char*)d_ws; float* out=(float*)d_out;
  u16* qkv=(u16*)ws; u16* phiq=(u16*)(ws+(8<<20)); u16* phik=(u16*)(ws+(16<<20)); float* kv=(float*)(ws+(24<<20)); u16* o=(u16*)(ws+(25<<20)); float* bu=(float*)(ws+(32<<20));
  k_qkv<<<dim3(1920/128,S),128,0,stream>>>(Ax,wqkv,qkv);
  k_phi<<<S*64/256,256,0,stream>>>(qkv,wphi,phiq,phik);
  k_kv<<<(32*840+63)/64,64,0,stream>>>(qkv,phik,kv);
  k_o<<<S*640/256,256,0,stream>>>(phiq,kv,o);
  k_a<<<dim3(5,S),128,0,stream>>>(o,wo,out);
  k_bu<<<dim3(5,S),128,0,stream>>>(Bx,Bre,Bim,bu);
  k_scan<<<10,64,0,stream>>>(lre,lim,ldt,bu);
  k_y<<<dim3(5,S),128,0,stream>>>(bu,Cre,Cim,Bx,dsk,out);
}
```

```cpp
#include <hip/hip_runtime.h>

typedef unsigned short u16;
typedef unsigned int u32;
typedef unsigned long long u64;
typedef short s16x4 __attribute__((ext_vector_type(4)));
typedef __bf16 bf16x8 __attribute__((ext_vector_type(8)));
typedef __bf16 bf16x2 __attribute__((ext_vector_type(2)));
typedef float f32x2 __attribute__((ext_vector_type(2)));
typedef float f32x4 __attribute__((ext_vector_type(4)));
typedef u32 u32x2 __attribute__((ext_vector_type(2)));
typedef u32 u32x4 __attribute__((ext_vector_type(4)));
typedef __amdgpu_buffer_rsrc_t rsrc_t;
#define LAS __attribute__((address_space(3)))
#define DI __device__ __forceinline__

constexpr u32 WS_EA = 0, WS_EB = 256;
constexpr u32 WS_OFLAG = 4096;
constexpr u32 WS_HFLAG = 20480;
constexpr u32 WS_EG = 65536;
constexpr u32 WS_KVF = 262144;
constexpr u32 WS_KVP = 524288;
constexpr u32 WS_OFR = 4194304;
constexpr u32 WS_HFR = 8388608;
constexpr u32 WS_USED = 13631488;
constexpr u32 OOB = 0x7fffff00u;
constexpr int SC1 = 16;
constexpr u32 SPIN_CAP = 1u << 20;

constexpr u32 STG = 36864;
constexpr u32 L_PHI = 0, L_V = 53248;
constexpr u32 QSTG = 26624;
constexpr u32 L_WPHI = 154624, L_ZERO = 159744;
constexpr u32 L_KVI = 102400;
constexpr u32 L_PAR = 147456, L_CAR = 150016, L_H = 102400;
constexpr u32 LDS_BYTES = 163840;

__shared__ __attribute__((aligned(16))) char smem[LDS_BYTES];

#define STAMP(i) do {} while (0)

DI rsrc_t mk(const void* p, u32 bytes) { return __builtin_amdgcn_make_buffer_rsrc((void*)p, (short)0, (int)bytes, 0x00020000); }
DI void dma16(rsrc_t r, u32 voff, u32 soff, u32 lds) {
  asm volatile("s_mov_b32 m0, %0\n\ts_nop 0\n\tbuffer_load_dwordx4 %1, %2, %3 offen lds" ::"s"(lds), "v"(voff), "s"(r), "s"(soff));
}
#define WAITV(n) asm volatile("s_waitcnt vmcnt(%0)" ::"i"(n) : "memory")
#define BAR() asm volatile("s_barrier" ::: "memory")
#define LBAR() asm volatile("s_waitcnt lgkmcnt(0)\n\ts_barrier" ::: "memory")
DI void waitv(int n) {
  if (n >= 13) WAITV(13); else if (n >= 11) WAITV(11); else if (n >= 10) WAITV(10); else if (n >= 8) WAITV(8); else if (n >= 6) WAITV(6);
  else if (n >= 5) WAITV(5); else if (n >= 4) WAITV(4); else if (n >= 3) WAITV(3); else WAITV(0);
}
DI u32x4 ld128(u32 a) {
  return *(const LAS u32x4*)((LAS char*)smem + a); }
DI u32x2 ld64(u32 a) { return *(const LAS u32x2*)((LAS char*)smem + a); }
DI u32 ld32(u32 a) { return *(const LAS u32*)((LAS char*)smem + a); }
DI void st128(u32 a, u32x4 v) { *(LAS u32x4*)((LAS char*)smem + a) = v; }
DI void st64(u32 a, u32x2 v) { *(LAS u32x2*)((LAS char*)smem + a) = v; }
DI void st32(u32 a, u32 v) { *(LAS u32*)((LAS char*)smem + a) = v; }
DI void st16(u32 a, u16 v) { *(LAS u16*)((LAS char*)smem + a) = v; }
DI u32x2 tr64(u32 a) {
  return __builtin_bit_cast(u32x2, __builtin_amdgcn_ds_read_tr16_b64_v4i16((LAS s16x4*)((LAS char*)smem + a))); }
DI u32x4 tr2(u32 a, u32 b) { u32x2 l = tr64(a), h = tr64(b); return u32x4{l[0], l[1], h[0], h[1]}; }
DI f32x4 mma(u32x4 a, u32x4 b, f32x4 c) {
  return __builtin_amdgcn_mfma_f32_16x16x32_bf16(__builtin_bit_cast(bf16x8, a), __builtin_bit_cast(bf16x8, b), c, 0, 0, 0);
}
DI f32x4 mma16(u32x2 a, u32x2 b, f32x4 c) {
  return __builtin_amdgcn_mfma_f32_16x16x16bf16_1k(__builtin_bit_cast(s16x4, a), __builtin_bit_cast(s16x4, b), c, 0, 0, 0);
}
DI u32 pk(float a, float b) { f32x2 v = {a, b}; return __builtin_bit_cast(u32, __builtin_convertvector(v, bf16x2)); }
DI float bf2f(u32 x) { return __uint_as_float(x << 16); }
DI float fmax2(float a, float b) { return __builtin_amdgcn_fmed3f(a, b, __builtin_inff()); }
DI u32 f2u(float x) { return __float_as_uint(x); }
DI float u2f(u32 x) { return __uint_as_float(x); }

template <bool MAX> DI void row_reduce2(float& a, float& b) {
  auto r = __builtin_amdgcn_permlane16_swap(f2u(a), f2u(b), false, false);
  float t = MAX ? fmax2(u2f(r[0]), u2f(r[1])) : u2f(r[0]) + u2f(r[1]);
  auto s = __builtin_amdgcn_permlane32_swap(f2u(t), f2u(t), false, false);
  t = MAX ? fmax2(u2f(s[0]), u2f(s[1])) : u2f(s[0]) + u2f(s[1]);
  auto q = __builtin_amdgcn_permlane16_swap(f2u(t), f2u(t), false, false);
  a = u2f(q[0]); b = u2f(q[1]);
}
DI void dual_exp(f32x4 t0, float t4, float (&ep)[5], float (&em)[5], float& sp, float& sm) {
  const float L = 1.44269504088896340736f;
  float t[5] = {t0[0] * L, t0[1] * L, t0[2] * L, t0[3] * L, t4 * L};
  float mx = fmaxf(fmaxf(fmaxf(t[0], t[1]), fmaxf(t[2], t[3])), t[4]);
  float nm = -fminf(fminf(fminf(t[0], t[1]), fminf(t[2], t[3])), t[4]);
  row_reduce2<true>(mx, nm);
#pragma unroll
  for (int i = 0; i < 5; ++i) { ep[i] = __builtin_amdgcn_exp2f(t[i] - mx); em[i] = __builtin_amdgcn_exp2f(-t[i] - nm); }
  sp = (ep[0] + ep[1]) + (ep[2] + ep[3]) + ep[4];
  sm = (em[0] + em[1]) + (em[2] + em[3]) + em[4];
  row_reduce2<false>(sp, sm);
}
DI u32x4 wphi_frag(int h, int ft, int off, int g, int q, int p) {
  const int d0 = 4 * g - off, d1 = d0 + 16;
  const u32 b = L_WPHI + h * 1280 + 32 * ft + 8 * p + q * 64;
  return tr2(d0 >= 0 ? b + d0 * 64 : L_ZERO, d1 <= 16 ? b + d1 * 64 : L_ZERO);
}
DI bool spin_more(u32& spins) { __builtin_amdgcn_s_sleep(1); return ++spins < SPIN_CAP; }
DI void wait_flags(rsrc_t rw, u32 off, u32 tagA, u32 tagB, int lane) {
  u32 spins = 0;
  for (;;) {
    asm volatile("" ::: "memory");
    u32x2 f = __builtin_amdgcn_raw_buffer_load_b64(rw, off + (lane & 7) * 8, 0, SC1);
    if (__all(f[0] == tagA && f[1] == tagB)) break;
    if (!spin_more(spins)) break;
  }
  asm volatile("" ::: "memory");
}

struct Ctx { int rg, cg, w, lane; u32 tagA, tagB; u64 epoch; };
DI void epoch_issue(Ctx& C, const char* p) { asm volatile("s_load_dwordx2 %0, %1, 0x0" : "=s"(C.epoch) : "s"(p)); }
DI void epoch_finish(Ctx& C) {
  asm volatile("s_waitcnt lgkmcnt(0)" : "+s"(C.epoch));
  C.epoch += 1; C.tagA = (u32)C.epoch; C.tagB = (u32)(C.epoch >> 32) + C.tagA * 0x9E3779B1u;
}

DI void chainA(const u16* __restrict__ Ax, const u16* __restrict__ Wqkv, const u16* __restrict__ Wphi, const u16* __restrict__ Wo,
               float* __restrict__ out, char* ws, Ctx& C
) {
  const int rg = C.rg, cg = C.cg, w = C.w, lane = C.lane, tid = w * 64 + lane;
  const int c = lane & 15, g = lane >> 4, q = (lane >> 2) & 3, p = lane & 3, tg = w & 3, u = w >> 2;
  const rsrc_t rx = mk(Ax + rg * 128 * 640, 128 * 1280), rw = mk(Wqkv, 640 * 3840), rws = mk(ws, WS_USED);
  u32 wv[5];
  {
    const rsrc_t rp = mk(Wphi + cg * 1600, 3200);
#pragma unroll
    for (int i = 0; i < 5; ++i) {
      const int idx = tid + 512 * i, h = idx / 640, r = idx - h * 640, d = r >> 5, col = r & 31;
      const int f = col < 16 ? col : 16 + ((col - 16) >> 2);
      const u32 off = (col < 16 || (col & 3) == 0) ? (u32)(h * 400 + d * 20 + f) * 2 : OOB;
      asm volatile("buffer_load_ushort %0, %1, %2, 0 offen" : "=v"(wv[i]) : "v"(off), "s"(rp));
    }
  }
  const u32 vx = (16 * w + (lane >> 3)) * 1280 + (((lane & 7) ^ (lane >> 3)) << 4);
  u32 vw[3];
#pragma unroll
  for (int i = 0; i < 3; ++i) {
    const int o = 1024 * (w + 8 * i) + 16 * lane, k = o / 320, cp = (o - 320 * k) >> 4;
    const int tj = (cp >> 1) ^ ((k >> 3) & 1), uu = tj >= 5, qq = 2 * (tj - 5 * uu) + (cp & 1);
    const int col = 640 + 80 * cg + 40 * uu + (qq < 5 ? 8 * qq : 640 + 8 * (qq - 5));
    vw[i] = k * 3840 + col * 2;
  }
  auto stage_kv = [&](int s) {
    const u32 b = (s & 3) * STG;
    dma16(rx, vx, s * 128, b + w * 2048);
    dma16(rx, vx, s * 128 + 10240, b + w * 2048 + 1024);
    dma16(rw, vw[0], s * 245760, b + 16384 + w * 1024);
    dma16(rw, vw[1], s * 245760, b + 24576 + w * 1024);
    if (w < 4) dma16(rw, vw[2], s * 245760, b + 32768 + w * 1024);
  };
  asm volatile("s_nop 4");
  stage_kv(0); stage_kv(1); stage_kv(2); stage_kv(3);
  epoch_issue(C, ws + WS_EA);
  const int nKV = w < 4 ? 5 : 4, nQ = w < 2 ? 4 : 3;
  u32 vq[2];
#pragma unroll
  for (int i = 0; i < 2; ++i) {
    const int o = 1024 * (w + 8 * i) + 16 * lane, r = o / 160, ch = (o - 160 * r) >> 4, k = r ^ (((r >> 3) & 1) << 2);
    vq[i] = k * 3840 + (80 * cg + 8 * ch) * 2;
  }
  auto qslot = [](int s) -> u32 { const int t = s >= 5 ? s - 5 : s; return t < 3 ? 74752 + t * QSTG : (t - 3) * QSTG; };
  auto stage_q = [&](int s) {
    const u32 b = qslot(s);
    dma16(rx, vx, s * 128, b + w * 2048);
    dma16(rx, vx, s * 128 + 10240, b + w * 2048 + 1024);
    dma16(rw, vq[0], s * 245760, b + 16384 + w * 1024);
    if (w < 2) dma16(rw, vq[1], s * 245760, b + 24576 + w * 1024);
  };
  STAMP(1);
  epoch_finish(C);
  if (w < 4) WAITV(20); else WAITV(16);
#pragma unroll
  for (int i = 0; i < 5; ++i) { asm volatile("" : "+v"(wv[i])); st16(L_WPHI + (tid + 512 * i) * 2, (u16)wv[i]); }
  if (tid < 16) st32(L_ZERO + tid * 4, 0);

  f32x4 acc[5][2];
#pragma unroll
  for (int j = 0; j < 5; ++j) acc[j][0] = acc[j][1] = f32x4{0, 0, 0, 0};
  {
    const u32 xb = (32 * tg + c) * 128 + ((g ^ (c & 7)) << 4);
    const int sb = (g & 1) * (u ? -32 : 32);
    const u32 wr = 16384 + (8 * g + q) * 320 + 8 * p + 160 * u, wE = wr + sb, wO = wr - sb;
    u32x4 fx[2][2], fw[2][5];
    auto rd = [&](int kk, u32 b) {
#pragma unroll
      for (int tt = 0; tt < 2; ++tt) fx[kk][tt] = ld128(b + (xb ^ (kk * 64)) + tt * 2048);
#pragma unroll
      for (int j = 0; j < 5; ++j) { const u32 a = b + ((j & 1) ? wO : wE) + 32 * j + kk * 10240; fw[kk][j] = tr2(a, a + 1280); }
    };
    auto mm = [&](int kk) {
#pragma unroll
      for (int j = 0; j < 5; ++j)
#pragma unroll
        for (int tt = 0; tt < 2; ++tt) acc[j][tt] = mma(fw[kk][j], fx[kk][tt], acc[j][tt]);
    };
    if (w < 4) WAITV(15); else WAITV(12);
    BAR();
    STAMP(2);
    rd(0, 0);
#pragma unroll 1
    for (int s = 0; s < 9; ++s) {
      rd(1, (s & 3) * STG);
      mm(0);
      waitv(s == 0 ? 2 * nKV : (s == 8 ? nQ : nKV));
      BAR();
      if (s >= 1 && s <= 6) stage_kv(s + 3);
      else if (s == 7) stage_q(0);
      else if (s == 8) { stage_q(1); stage_q(2); }
      rd(0, ((s + 1) & 3) * STG);
      mm(1);
    }
    rd(1, STG);
    mm(0);
    mm(1);
  }
  STAMP(3);
  {
    u32x2 kp[3][2];
#pragma unroll
    for (int j = 0; j < 3; ++j)
#pragma unroll
      for (int tt = 0; tt < 2; ++tt) kp[j][tt] = u32x2{pk(acc[j][tt][0], acc[j][tt][1]), pk(acc[j][tt][2], acc[j][tt][3])};
    u32x2 ph[2][2][3];
#pragma unroll
    for (int hh = 0; hh < 2; ++hh) {
      const u32x4 a0 = wphi_frag(2 * u + hh, 0, 4 * hh, g, q, p), a1 = wphi_frag(2 * u + hh, 1, 4 * hh, g, q, p);
#pragma unroll
      for (int tt = 0; tt < 2; ++tt) {
        const u32x4 b = {kp[hh][tt][0], kp[hh][tt][1], kp[hh + 1][tt][0], kp[hh + 1][tt][1]};
        const f32x4 t0 = mma(a0, b, f32x4{0, 0, 0, 0}), t1 = mma(a1, b, f32x4{0, 0, 0, 0});
        float ep[5], em[5], sp, sm;
        dual_exp(t0, t1[0], ep, em, sp, sm);
        const float rp = __builtin_amdgcn_rcpf(sp), rm = __builtin_amdgcn_rcpf(sm);
        ph[hh][tt][0] = u32x2{pk(ep[0] * rp, ep[1] * rp), pk(ep[2] * rp, ep[3] * rp)};
        ph[hh][tt][1] = u32x2{pk(em[0] * rm, em[1] * rm), pk(em[2] * rm, em[3] * rm)};
        ph[hh][tt][2] = u32x2{pk(ep[4] * rp, em[4] * rm), 0u};
      }
    }
    BAR();
#pragma unroll
    for (int tt = 0; tt < 2; ++tt) {
      const int row = 32 * tg + 16 * tt + c;
#pragma unroll
      for (int hh = 0; hh < 2; ++hh)
#pragma unroll
        for (int i = 0; i < 3; ++i) st64(L_PHI + (2 * u + hh) * 13312 + row * 104 + 8 * g + 32 * i, ph[hh][tt][i]);
      const u32 vb = L_V + row * 168;
      const u32x2 v2 = {pk(acc[2][tt][0], acc[2][tt][1]), pk(acc[2][tt][2], acc[2][tt][3])};
      st64(g < 2 ? vb + 160 : vb + 80 * u + 8 * g - 16, g < 2 ? u32x2{0x3f803f80u, 0x3f803f80u} : v2);
#pragma unroll
      for (int j = 3; j < 5; ++j)
        st64(vb + 80 * u + 32 * j + 8 * g - 80, u32x2{pk(acc[j][tt][0], acc[j][tt][1]), pk(acc[j][tt][2], acc[j][tt][3])});
    }
    LBAR();
  }
  STAMP(4);
  {
    const int j = w >> 1, dt = w & 1;
    const u32 ab = L_PHI + j * 13312 + (4 * g + q) * 104 + 8 * p;
    const u32 bb = L_V + (4 * g + q) * 168 + (dt == 0 ? 40 * j + 8 * p : (p == j ? 40 * j + 32 : 160));
    f32x4 kv[3] = {{0, 0, 0, 0}, {0, 0, 0, 0}, {0, 0, 0, 0}};
#pragma unroll
    for (int ks = 0; ks < 4; ++ks) {
      const u32x4 b = tr2(bb + ks * 5376, bb + ks * 5376 + 2688);
#pragma unroll
      for (int ft = 0; ft < 3; ++ft) kv[ft] = mma(tr2(ab + ks * 3328 + 32 * ft, ab + ks * 3328 + 32 * ft + 1664), b, kv[ft]);
    }
    const int entry = dt == 0 ? lane : ((c >> 2) == j ? 64 + (c & 3) + 4 * g : (c == 4 * ((j + 1) & 3) ? 80 + g : -1));
    const u32 off = entry >= 0 ? WS_KVP + (u32)((cg * 16 + rg) * 1680 + j * 420 + entry) * 16 : OOB;
#pragma unroll
    for (int i = 0; i < 5; ++i)
      __builtin_amdgcn_raw_buffer_store_b128(u32x4{f2u(kv[i >> 1][2 * (i & 1)]), C.tagA, f2u(kv[i >> 1][2 * (i & 1) + 1]), C.tagB}, rws, off,
                                             i * 84 * 16, SC1);
  }
  STAMP(5);
  f32x4 qa[5];
#pragma unroll
  for (int j = 0; j < 5; ++j) qa[j] = f32x4{0, 0, 0, 0};
  {
    const u32 xb = (16 * w + c) * 128 + ((g ^ (c & 7)) << 4);
    const int r1 = 8 * g + 4 * (g & 1) + q;
    const u32 w1 = 16384 + r1 * 160 + 8 * p, w2 = 16384 + (r1 ^ 4) * 160 + 8 * p;
    u32x4 fx[2], fw[2][5];
    auto rd = [&](int kk, u32 b) {
      fx[kk] = ld128(b + (xb ^ (kk * 64)));
#pragma unroll
      for (int j = 0; j < 5; ++j) fw[kk][j] = tr2(b + w1 + 32 * j + kk * 5120, b + w2 + 32 * j + kk * 5120);
    };
    auto mm = [&](int kk) {
#pragma unroll
      for (int j = 0; j < 5; ++j) qa[j] = mma(fw[kk][j], fx[kk], qa[j]);
    };
    waitv(2 * nQ + 5);
    BAR();
    stage_q(3);
    rd(0, qslot(0));
#pragma unroll 1
    for (int s = 0; s < 9; ++s) {
      rd(1, qslot(s));
      mm(0);
      waitv(s < 2 ? 2 * nQ + 5 : (s < 7 ? 2 * nQ : (s == 7 ? nQ : 0)));
      BAR();
      if (s <= 5) stage_q(s + 4);
      rd(0, qslot(s + 1));
      mm(1);
    }
    rd(1, qslot(9));
    mm(0);
    mm(1);
  }
  STAMP(6);
  const u32 roff = tid < 420 ? WS_KVP + (u32)((cg * 16 + 4 * (tid & 3)) * 1680 + 105 * rg + (tid >> 2)) * 16 : OOB;
  u32x4 pv[4];
#pragma unroll
  for (int i = 0; i < 4; ++i) pv[i] = __builtin_amdgcn_raw_buffer_load_b128(rws, roff, i * 1680 * 16, SC1);
  u32x4 pq0[4]; u32 pq1[4]; float eps[4];
  {
    u32x2 qp[5];
#pragma unroll
    for (int j = 0; j < 5; ++j) qp[j] = u32x2{pk(qa[j][0], qa[j][1]), pk(qa[j][2], qa[j][3])};
#pragma unroll
    for (int j = 0; j < 4; ++j) {
      const u32x4 b = {qp[j][0], qp[j][1], qp[j + 1][0], qp[j + 1][1]};
      const f32x4 t0 = mma(wphi_frag(j, 0, 4 * j, g, q, p), b, f32x4{0, 0, 0, 0}), t1 = mma(wphi_frag(j, 1, 4 * j, g, q, p), b, f32x4{0, 0, 0, 0});
      float ep[5], em[5], sp, sm;
      dual_exp(t0, t1[0], ep, em, sp, sm);
      const float r = sp * __builtin_amdgcn_rcpf(sm);
      pq0[j] = u32x4{pk(ep[0], ep[1]), pk(ep[2], ep[3]), pk(em[0] * r, em[1] * r), pk(em[2] * r, em[3] * r)};
      pq1[j] = pk(ep[4], em[4] * r);
      eps[j] = 1e-6f * sp;
    }
  }
  STAMP(7);
  {
    u32 spins = 0;
    for (;;) {
      bool ok = true;
#pragma unroll
      for (int i = 0; i < 4; ++i) ok &= pv[i][1] == C.tagA && pv[i][3] == C.tagB;
      if (__all(ok || tid >= 420)) break;
      if (!spin_more(spins)) break;
      asm volatile("" ::: "memory");
#pragma unroll
      for (int i = 0; i < 4; ++i) pv[i] = __builtin_amdgcn_raw_buffer_load_b128(rws, roff, i * 1680 * 16, SC1);
    }
    float s0 = (u2f(pv[0][0]) + u2f(pv[1][0])) + (u2f(pv[2][0]) + u2f(pv[3][0]));
    float s1 = (u2f(pv[0][2]) + u2f(pv[1][2])) + (u2f(pv[2][2]) + u2f(pv[3][2]));
    s0 += u2f(__builtin_amdgcn_mov_dpp(f2u(s0), 0xB1, 0xf, 0xf, true)); s1 += u2f(__builtin_amdgcn_mov_dpp(f2u(s1), 0xB1, 0xf, 0xf, true));
    s0 += u2f(__builtin_amdgcn_mov_dpp(f2u(s0), 0x4E, 0xf, 0xf, true)); s1 += u2f(__builtin_amdgcn_mov_dpp(f2u(s1), 0x4E, 0xf, 0xf, true));
    const u32 fo = (tid < 420 && (tid & 3) == 0) ? WS_KVF + (u32)(cg * 1680 + 105 * rg + (tid >> 2)) * 16 : OOB;
    __builtin_amdgcn_raw_buffer_store_b128(u32x4{pk(s0, s1), C.tagA, 0u, C.tagB}, rws, fo, 0, SC1);
  }
  STAMP(8);
  BAR();
  {
    const rsrc_t ro = mk(Wo, 640 * 1280);
#pragma unroll 1
    for (int P = w; P < 100; P += 8) {
      const int o = 1024 * P + 16 * lane, k = o / 160, ch = (o - 160 * k) >> 4;
      dma16(ro, k * 1280 + (80 * cg + 8 * ch) * 2, 0, P * 1024);
    }
  }
  {
    u32x4 fv[4];
    u32 spins = 0;
    for (;;) {
      asm volatile("" ::: "memory");
      bool ok = true;
#pragma unroll
      for (int i = 0; i < 4; ++i) {
        const int ga = tid + 512 * i;
        fv[i] = __builtin_amdgcn_raw_buffer_load_b128(rws, ga < 1680 ? WS_KVF + (u32)(cg * 1680 + ga) * 16 : OOB, 0, SC1);
      }
#pragma unroll
      for (int i = 0; i < 4; ++i) ok &= (fv[i][1] == C.tagA && fv[i][3] == C.tagB) || tid + 512 * i >= 1680;
      if (__all(ok)) break;
      if (!spin_more(spins)) break;
    }
#pragma unroll
    for (int i = 0; i < 4; ++i) {
      const int ga = tid + 512 * i, j = ga / 420, r = ga - 420 * j, i5 = r / 84, e = r - 84 * i5;
      if (ga < 1680) st32(L_KVI + j * 1680 + (i5 < 4 ? e * 16 + 4 * i5 : 1344 + e * 4), fv[i][0]);
    }
    LBAR();
  }
  STAMP(9);
  {
    u32x2 oq[4], ex = {0u, 0u};
#pragma unroll
    for (int j = 0; j < 4; ++j) {
      const int e1 = (c >> 2) == j ? 64 + (c & 3) + 4 * g : 80 + g;
      const u32 kb = L_KVI + j * 1680;
      f32x4 t0 = mma(ld128(kb + lane * 16), pq0[j], f32x4{0, 0, 0, 0});
      f32x4 t1 = mma(ld128(kb + e1 * 16), pq0[j], f32x4{0, 0, 0, 0});
      t0 = mma16(u32x2{ld32(kb + 1344 + lane * 4), 0u}, u32x2{pq1[j], 0u}, t0);
      t1 = mma16(u32x2{ld32(kb + 1344 + e1 * 4), 0u}, u32x2{pq1[j], 0u}, t1);
      auto sw = __builtin_amdgcn_permlane16_swap(f2u(t1[0]), f2u(t1[0]), false, false);
      const float r = __builtin_amdgcn_rcpf(u2f(sw[(j & 1) ^ 1]) + eps[j]);
      oq[j] = u32x2{pk(t0[0] * r, t0[1] * r), pk(t0[2] * r, t0[3] * r)};
      const u32x2 e = {pk(t1[0] * r, t1[1] * r), pk(t1[2] * r, t1[3] * r)};
      ex = g == j ? e : ex;
    }
    const u32 ob = WS_OFR + (u32)((rg * 8 + w) * 8 + cg) * 2560;
    __builtin_amdgcn_raw_buffer_store_b128(u32x4{oq[0][0], oq[0][1], oq[1][0], oq[1][1]}, rws, ob + lane * 16, 0, SC1);
    __builtin_amdgcn_raw_buffer_store_b128(u32x4{oq[2][0], oq[2][1], oq[3][0], oq[3][1]}, rws, ob + lane * 16, 1024, SC1);
    __builtin_amdgcn_raw_buffer_store_b64(ex, rws, ob + lane * 8, 2048, SC1);
    WAITV(0);
    __builtin_amdgcn_raw_buffer_store_b64(u32x2{C.tagA, C.tagB}, rws, lane == 0 ? WS_OFLAG + (u32)(rg * 8 + w) * 128 + cg * 8 : OOB, 0, SC1);
  }
  STAMP(10);
  BAR();
  wait_flags(rws, WS_OFLAG + (u32)(rg * 8 + w) * 128, C.tagA, C.tagB, lane);
  if (rg == 0 && cg == 0 && w == 0) __builtin_amdgcn_raw_buffer_store_b64(u32x2{(u32)C.epoch, (u32)(C.epoch >> 32)}, rws, lane == 0 ? WS_EA : OOB, 0, SC1);
  STAMP(11);
  {
    u32x4 f0[8], f1[8]; u32x2 fe[8];
    const u32 ob = WS_OFR + (u32)(rg * 8 + w) * 8 * 2560;
#pragma unroll
    for (int i = 0; i < 8; ++i) {
      f0[i] = __builtin_amdgcn_raw_buffer_load_b128(rws, ob + lane * 16, i * 2560, SC1);
      f1[i] = __builtin_amdgcn_raw_buffer_load_b128(rws, ob + lane * 16, i * 2560 + 1024, SC1);
      fe[i] = __builtin_amdgcn_raw_buffer_load_b64(rws, ob + lane * 8, i * 2560 + 2048, SC1);
    }
    f32x4 a[5];
#pragma unroll
    for (int j = 0; j < 5; ++j) a[j] = f32x4{0, 0, 0, 0};
    const u32 b0 = (4 * g + q) * 160 + 8 * p, bx = (20 * g + 16 + q) * 160 + 8 * p;
#pragma unroll
    for (int i = 0; i < 8; ++i) {
#pragma unroll
      for (int j = 0; j < 5; ++j) a[j] = mma(tr2(b0 + i * 12800 + 32 * j, b0 + i * 12800 + 3200 + 32 * j), f0[i], a[j]);
#pragma unroll
      for (int j = 0; j < 5; ++j) a[j] = mma(tr2(b0 + i * 12800 + 6400 + 32 * j, b0 + i * 12800 + 9600 + 32 * j), f1[i], a[j]);
      if (i & 1) {
        const u32x4 b = {fe[i - 1][0], fe[i - 1][1], fe[i][0], fe[i][1]};
#pragma unroll
        for (int j = 0; j < 5; ++j) a[j] = mma(tr2(bx + (i - 1) * 12800 + 32 * j, bx + i * 12800 + 32 * j), b, a[j]);
      }
    }
    STAMP(12);
    float* op = out + (size_t)(128 * rg + 16 * w + c) * 1280 + 80 * cg + 4 * g;
#pragma unroll
    for (int j = 0; j < 5; ++j) *(f32x4*)(op + 16 * j) = a[j];
  }
}

template <int NT, class FS, class FM>
DI void b_phase1(int js0, int tg, int lane, int w, int nS, FS& stage, FM& mid
) {
  const int c = lane & 15, g = lane >> 4;
  f32x4 acc[NT][2][2];
#pragma unroll
  for (int j = 0; j < NT; ++j) acc[j][0][0] = acc[j][0][1] = acc[j][1][0] = acc[j][1][1] = f32x4{0, 0, 0, 0};
  {
    const u32 sw = (g ^ (c & 7)) << 4, xb = (32 * tg + c) * 128 + sw, wb = 16384 + (16 * js0 + c) * 128 + sw;
    u32x4 fx[2][2], fw[2][NT][2];
    auto rd = [&](int kk, u32 b) {
#pragma unroll
      for (int m = 0; m < 2; ++m) fx[kk][m] = ld128(b + (xb ^ (kk * 64)) + m * 2048);
#pragma unroll
      for (int j = 0; j < NT; ++j)
#pragma unroll
        for (int z = 0; z < 2; ++z) fw[kk][j][z] = ld128(b + (wb ^ (kk * 64)) + j * 2048 + z * 10240);
    };
    auto mm = [&](int kk) {
#pragma unroll
      for (int j = 0; j < NT; ++j)
#pragma unroll
        for (int z = 0; z < 2; ++z)
#pragma unroll
          for (int m = 0; m < 2; ++m) acc[j][m][z] = mma(fx[kk][m], fw[kk][j][z], acc[j][m][z]);
    };
    if (w < 4) WAITV(15); else WAITV(12);
    LBAR();
    STAMP(2);
    rd(0, 0);
#pragma unroll 1
    for (int s = 0; s < 9; ++s) {
      rd(1, (s & 3) * STG);
      mm(0);
      waitv(s == 0 ? 2 * nS : (s == 8 ? 0 : nS));
      BAR();
      if (s >= 1 && s <= 6) stage(s + 3);
      rd(0, ((s + 1) & 3) * STG);
      mm(1);
    }
    rd(1, STG);
    mm(0);
    mm(1);
  }
  STAMP(3);
  f32x4 par[NT];
#pragma unroll
  for (int j = 0; j < NT; ++j) {
    par[j] = __builtin_bit_cast(f32x4, ld128(L_PAR + (16 * (js0 + j) + c) * 32));
    const float lr = par[j][0], li = par[j][1];
    float er = acc[j][0][0][0], ei = acc[j][0][1][0];
#pragma unroll
    for (int t = 1; t < 8; ++t) {
      const float nr = fmaf(lr, er, fmaf(-li, ei, acc[j][t >> 2][0][t & 3])), ni = fmaf(lr, ei, fmaf(li, er, acc[j][t >> 2][1][t & 3]));
      er = nr; ei = ni;
    }
    st64(L_CAR + ((4 * tg + g) * 80 + 16 * (js0 + j) + c) * 8, u32x2{f2u(er), f2u(ei)});
  }
  LBAR();
  STAMP(4);
  mid();
  LBAR();
  STAMP(6);
#pragma unroll
  for (int j = 0; j < NT; ++j) {
    const float lr = par[j][0], li = par[j][1], cr = par[j][2], ci = par[j][3];
    const u32x2 h0 = ld64(L_CAR + ((4 * tg + g) * 80 + 16 * (js0 + j) + c) * 8);
    float er = u2f(h0[0]), ei = u2f(h0[1]), yr[8], yi[8];
#pragma unroll
    for (int t = 0; t < 8; ++t) {
      const float nr = fmaf(lr, er, fmaf(-li, ei, acc[j][t >> 2][0][t & 3])), ni = fmaf(lr, ei, fmaf(li, er, acc[j][t >> 2][1][t & 3]));
      er = nr; ei = ni;
      yr[t] = fmaf(cr, er, -ci * ei); yi[t] = fmaf(-cr, ei, -ci * er);
    }
    const u32 hb = L_H + (32 * (js0 + j) + c) * 272 + 64 * tg + 16 * g;
    st128(hb, u32x4{pk(yr[0], yr[1]), pk(yr[2], yr[3]), pk(yr[4], yr[5]), pk(yr[6], yr[7])});
    st128(hb + 16 * 272, u32x4{pk(yi[0], yi[1]), pk(yi[2], yi[3]), pk(yi[4], yi[5]), pk(yi[6], yi[7])});
  }
  LBAR();
}

DI void chainB(const u16* __restrict__ Bx, const u16* __restrict__ Lre, const u16* __restrict__ Lim, const u16* __restrict__ Bre,
               const u16* __restrict__ Bim, const u16* __restrict__ Cre, const u16* __restrict__ Cim, const u16* __restrict__ Ldt,
               const u16* __restrict__ Dsk, float* __restrict__ out, char* ws, Ctx& C
) {
  const int rg = C.rg, cg = C.cg, w = C.w, lane = C.lane, tid = w * 64 + lane;
  const int c = lane & 15, g = lane >> 4, q = (lane >> 2) & 3, p = lane & 3, tg = w & 3;
  const rsrc_t rx = mk(Bx + rg * 128 * 640, 128 * 1280), rws = mk(ws, WS_USED);
  u32 plr, pli, pdt;
  {
    const u32 off = tid < 80 ? (u32)(80 * cg + tid) * 2 : OOB;
    asm volatile("buffer_load_ushort %0, %1, %2, 0 offen" : "=v"(plr) : "v"(off), "s"(mk(Lre, 1280)));
    asm volatile("buffer_load_ushort %0, %1, %2, 0 offen" : "=v"(pli) : "v"(off), "s"(mk(Lim, 1280)));
    asm volatile("buffer_load_ushort %0, %1, %2, 0 offen" : "=v"(pdt) : "v"(off), "s"(mk(Ldt, 1280)));
  }
  const int l8 = lane >> 3;
  const u32 sw = ((lane & 7) ^ l8) << 4;
  const u32 vx = (32 * (w >> 1) + 8 * (l8 >> 2) + 4 * (w & 1) + (l8 & 3)) * 1280 + sw;
  const u32 vw = l8 * 1280 + sw;
  auto wtile = [&](const u16* re, const u16* im, int kt, u32 dst) {
    const u32 r0 = (80 * cg + 8 * w) * 1280 + kt * 128;
    dma16(mk(re, 640 * 1280), vw, r0, dst + w * 1024);
    dma16(mk(w < 2 ? re : im, 640 * 1280), vw, w < 2 ? r0 + 64 * 1280 : r0 - 16 * 1280, dst + 8192 + w * 1024);
    if (w < 4) dma16(mk(im, 640 * 1280), vw, r0 + 48 * 1280, dst + 16384 + w * 1024);
  };
  auto stage = [&](int s) {
    const u32 b = (s & 3) * STG;
    dma16(rx, vx, s * 128, b + w * 2048);
    dma16(rx, vx, s * 128 + 20480, b + w * 2048 + 1024);
    wtile(Bre, Bim, s, b + 16384);
  };
  asm volatile("s_nop 4");
  stage(0); stage(1); stage(2); stage(3);
  epoch_issue(C, ws + WS_EB);
  const int nS = w < 4 ? 5 : 4;
  STAMP(1);
  epoch_finish(C);
  if (w < 4) WAITV(20); else WAITV(16);
  asm volatile("" : "+v"(plr), "+v"(pli), "+v"(pdt));
  float l8r = 0.f, l8i = 0.f, lLr = 0.f, lLi = 0.f;
  if (w < 2) {
    const float lr = bf2f(plr), li = bf2f(pli), dt = bf2f(pk(expf(bf2f(pdt)), 0.f) & 0xffffu);
    float sn, cs;
    sincosf(dt * li, &sn, &cs);
    const float e = expf(dt * lr), br = e * cs, bi = e * sn, nr = br - 1.f, dd = 2.f / (lr * lr + li * li);
    float ar = br, ai = bi;
#pragma unroll
    for (int i = 0; i < 7; ++i) {
      const float t = ar * ar - ai * ai; ai = 2.f * ar * ai; ar = t;
      if (i == 2) { l8r = ar; l8i = ai; }
    }
    lLr = ar; lLi = ai;
    if (tid < 80) st128(L_PAR + tid * 32, u32x4{f2u(br), f2u(bi), f2u((nr * lr + bi * li) * dd), f2u((bi * lr - nr * li) * dd)});
  }
  f32x4 a[5];
  auto mid = [&]() {
#pragma unroll 1
    for (int k = 0; k < 5; ++k) wtile(Cre, Cim, k, k * 20480);
    if (w < 2) {
      const int s = tid < 80 ? tid : 0;
      float er[16], ei[16], pr = 0.f, pi = 0.f;
#pragma unroll
      for (int j = 0; j < 16; ++j) { const u32x2 v = ld64(L_CAR + (j * 80 + s) * 8); er[j] = u2f(v[0]); ei[j] = u2f(v[1]); }
#pragma unroll
      for (int j = 0; j < 16; ++j) { const float t = fmaf(l8r, pr, fmaf(-l8i, pi, er[j])); pi = fmaf(l8r, pi, fmaf(l8i, pr, ei[j])); pr = t; }
      const u32 eb = tid < 80 ? WS_EG + (u32)(cg * 16 * 80 + tid) * 16 : OOB;
      __builtin_amdgcn_raw_buffer_store_b128(u32x4{f2u(pr), C.tagA, f2u(pi), C.tagB}, rws, eb, rg * 1280, SC1);
      u32x4 ev[15];
      u32 spins = 0;
      for (;;) {
        asm volatile("" ::: "memory");
        bool ok = true;
#pragma unroll
        for (int j = 0; j < 15; ++j) ev[j] = __builtin_amdgcn_raw_buffer_load_b128(rws, j < rg ? eb : OOB, j * 1280, SC1);
#pragma unroll
        for (int j = 0; j < 15; ++j) ok &= (ev[j][1] == C.tagA && ev[j][3] == C.tagB) || j >= rg;
        if (__all(ok || tid >= 80)) break;
        if (!spin_more(spins)) break;
      }
      STAMP(5);
      float hr = 0.f, hi = 0.f;
#pragma unroll
      for (int j = 0; j < 15; ++j)
        if (j < rg) { const float t = fmaf(lLr, hr, fmaf(-lLi, hi, u2f(ev[j][0]))); hi = fmaf(lLr, hi, fmaf(lLi, hr, u2f(ev[j][2]))); hr = t; }
#pragma unroll
      for (int j = 0; j < 16; ++j) {
        if (tid < 80) st64(L_CAR + (j * 80 + s) * 8, u32x2{f2u(hr), f2u(hi)});
        const float t = fmaf(l8r, hr, fmaf(-l8i, hi, er[j])); hi = fmaf(l8r, hi, fmaf(l8i, hr, ei[j])); hr = t;
      }
    }
  };
  if (w < 4) b_phase1<3>(0, tg, lane, w, nS, stage, mid); else b_phase1<2>(3, tg, lane, w, nS, stage, mid);
  STAMP(7);
  {
    const u32 hb = L_H + (8 * g + q) * 272 + 32 * w + 8 * p;
    const u32 ob = WS_HFR + (u32)((rg * 8 + w) * 40 + 5 * cg) * 1024 + lane * 16;
#pragma unroll
    for (int j = 0; j < 5; ++j) __builtin_amdgcn_raw_buffer_store_b128(tr2(hb + j * 8704, hb + j * 8704 + 1088), rws, ob, j * 1024, SC1);
    const rsrc_t rd = mk(Dsk, 1280);
#pragma unroll
    for (int j = 0; j < 5; ++j) {
      const u32x2 xv = __builtin_amdgcn_raw_buffer_load_b64(rx, (16 * w + c) * 1280 + (80 * cg + 4 * g) * 2, 32 * j, 0);
      const u32x2 dv = __builtin_amdgcn_raw_buffer_load_b64(rd, (80 * cg + 4 * g) * 2, 32 * j, 0);
      const u32 p0 = pk(bf2f(xv[0]) * bf2f(dv[0]), u2f(xv[0] & 0xffff0000u) * u2f(dv[0] & 0xffff0000u));
      const u32 p1 = pk(bf2f(xv[1]) * bf2f(dv[1]), u2f(xv[1] & 0xffff0000u) * u2f(dv[1] & 0xffff0000u));
      a[j] = f32x4{bf2f(p0), u2f(p0 & 0xffff0000u), bf2f(p1), u2f(p1 & 0xffff0000u)};
    }
    WAITV(0);
    __builtin_amdgcn_raw_buffer_store_b64(u32x2{C.tagA, C.tagB}, rws, lane == 0 ? WS_HFLAG + (u32)(rg * 8 + w) * 128 + cg * 8 : OOB, 0, SC1);
  }
  STAMP(8);
  BAR();
#pragma unroll 1
  for (int k = 5; k < 8; ++k) wtile(Cre, Cim, k, k * 20480);
  wait_flags(rws, WS_HFLAG + (u32)(rg * 8 + w) * 128, C.tagA, C.tagB, lane);
  if (rg == 15 && cg == 0 && w == 0) __builtin_amdgcn_raw_buffer_store_b64(u32x2{(u32)C.epoch, (u32)(C.epoch >> 32)}, rws, lane == 0 ? WS_EB : OOB, 0, SC1);
  STAMP(9);
  {
    u32x4 f[40];
    const u32 ob = WS_HFR + (u32)(rg * 8 + w) * 40 * 1024 + lane * 16;
#pragma unroll
    for (int i = 0; i < 40; ++i) f[i] = __builtin_amdgcn_raw_buffer_load_b128(rws, ob + (i & 3) * 1024, (i >> 2) * 4096, SC1);
    const u32 cb = (g >> 1) * 10240 + c * 128 + (((g & 1) ^ (c & 7)) << 4);
#pragma unroll
    for (int k = 0; k < 10; ++k) {
      if (k == 2) {
        BAR();
        wtile(Cre, Cim, 8, 0); wtile(Cre, Cim, 9, 20480);
      }
      if (k == 8) { WAITV(0); BAR(); }
#pragma unroll
      for (int i = 0; i < 4; ++i)
#pragma unroll
        for (int j = 0; j < 5; ++j) a[j] = mma(ld128((cb ^ (32 * i)) + (k & 7) * 20480 + j * 2048), f[4 * k + i], a[j]);
    }
    STAMP(10);
    float* op = out + (size_t)(128 * rg + 16 * w + c) * 1280 + 640 + 80 * cg + 4 * g;
#pragma unroll
    for (int j = 0; j < 5; ++j) *(f32x4*)(op + 16 * j) = a[j];
  }
}

__global__ __launch_bounds__(512, 2) void fused_kernel(const u16* Ax, const u16* Wqkv, const u16* Wphi, const u16* Wo, const u16* Bx,
                                                       const u16* Lre, const u16* Lim, const u16* Bre, const u16* Bim, const u16* Cre,
                                                       const u16* Cim, const u16* Ldt, const u16* Dsk, float* out, char* ws) {
  const int bid = blockIdx.x, xcd = bid & 7, local = ((xcd & 3) << 5) | (bid >> 3);
  Ctx C;
  C.rg = local >> 3; C.cg = local & 7;
  C.w = __builtin_amdgcn_readfirstlane(threadIdx.x >> 6); C.lane = threadIdx.x & 63;
  asm volatile("" ::"s"(Ax), "s"(Wqkv), "s"(Wphi), "s"(Wo), "s"(Bx), "s"(Lre), "s"(Lim), "s"(Bre), "s"(Bim), "s"(Cre), "s"(Cim), "s"(Ldt), "s"(Dsk), "s"(out), "s"(ws));
  if (xcd < 4) chainA(Ax, Wqkv, Wphi, Wo, out, ws, C); else chainB(Bx, Lre, Lim, Bre, Bim, Cre, Cim, Ldt, Dsk, out, ws, C);
}

extern "C" void kernel_launch(void* const* d_in, const int* in_sizes, int n_in, void* d_out, int out_size, void* d_ws, size_t ws_size,
                              hipStream_t stream) {
  fused_kernel<<<256, 512, 0, stream>>>((const u16*)d_in[0], (const u16*)d_in[1], (const u16*)d_in[2], (const u16*)d_in[3], (const u16*)d_in[4],
                                        (const u16*)d_in[5], (const u16*)d_in[6], (const u16*)d_in[7], (const u16*)d_in[8], (const u16*)d_in[9],
                                        (const u16*)d_in[10], (const u16*)d_in[11], (const u16*)d_in[12], (float*)d_out, (char*)d_ws);
}
```

```cpp
#define X 1
#include <hip/hip_runtime.h>

typedef unsigned short u16;
typedef unsigned int u32;
typedef unsigned long long u64;
typedef short s16x4 __attribute__((ext_vector_type(4)));
typedef __bf16 bf16x8 __attribute__((ext_vector_type(8)));
typedef __bf16 bf16x2 __attribute__((ext_vector_type(2)));
typedef float f32x2 __attribute__((ext_vector_type(2)));
typedef float f32x4 __attribute__((ext_vector_type(4)));
typedef u32 u32x2 __attribute__((ext_vector_type(2)));
typedef u32 u32x4 __attribute__((ext_vector_type(4)));
typedef __amdgpu_buffer_rsrc_t rsrc_t;
#define LAS __attribute__((address_space(3)))
#define DI __device__ __forceinline__

constexpr u32 WS_EA = 0, WS_EB = 256;
constexpr u32 WS_OFLAG = 4096;
constexpr u32 WS_HFLAG = 20480;
constexpr u32 WS_EG = 65536;
constexpr u32 WS_KVF = 262144;
constexpr u32 WS_KVP = 524288;
constexpr u32 WS_OFR = 4194304;
constexpr u32 WS_HFR = 8388608;
constexpr u32 WS_USED = 13631488;
constexpr u32 OOB = 0x7fffff00u;
constexpr int SC1 = 16;
constexpr u32 SPIN_CAP = 1u << 16;

constexpr u32 STG = 36864;
constexpr u32 L_PHI = 0, L_V = 53248;
constexpr u32 L_WQ = 74752;
constexpr u32 L_WPHI = 158656, L_ZERO = 163776;
constexpr u32 L_WO0 = 20480, L_WO1 = 74752;
constexpr u32 L_KVI = 125952;
constexpr u32 L_PAR = 147456, L_CAR = 150016, L_H = 0;
constexpr u32 L_SINKA = 156672, L_SINKB = 160256;
constexpr u32 LDS_BYTES = 163840;

__shared__ __attribute__((aligned(16))) char smem[LDS_BYTES];

#define STAMP(i) do {} while (0)

DI rsrc_t mk(const void* p, u32 bytes) { return __builtin_amdgcn_make_buffer_rsrc((void*)p, (short)0, (int)bytes, 0x00020000); }
DI void dma16(rsrc_t r, u32 voff, u32 soff, u32 lds) {
  asm volatile("s_mov_b32 m0, %0\n\ts_nop 0\n\tbuffer_load_dwordx4 %1, %2, %3 offen lds" ::"s"(__builtin_amdgcn_readfirstlane(lds)), "v"(voff), "s"(r), "s"(soff));
}
DI void dma16nt(rsrc_t r, u32 voff, u32 soff, u32 lds) {
  asm volatile("s_mov_b32 m0, %0\n\ts_nop 0\n\tbuffer_load_dwordx4 %1, %2, %3 offen nt lds" ::"s"(__builtin_amdgcn_readfirstlane(lds)), "v"(voff), "s"(r), "s"(soff));
}
DI void touch(rsrc_t r, u32 voff, u32 lds) {
  asm volatile("s_mov_b32 m0, %0\n\ts_nop 0\n\tbuffer_load_dword %1, %2, 0 offen lds" ::"s"(lds), "v"(voff), "s"(r));
}
DI void dma16m(rsrc_t r, u32 voff, u32 soff, u32 lds, int mask) {
  asm volatile("s_mov_b32 m0, %0\n\ts_or_b32 exec_lo, %4, 1\n\ts_mov_b32 exec_hi, %4\n\tbuffer_load_dwordx4 %1, %2, %3 offen lds\n\ts_mov_b64 exec, -1" ::"s"(__builtin_amdgcn_readfirstlane(lds)), "v"(voff), "s"(r), "s"(soff), "s"(mask) : "scc");
}
#define WAITV(n) asm volatile("s_waitcnt vmcnt(%0)" ::"i"(n) : "memory")
#define BAR() asm volatile("s_barrier" ::: "memory")
#define LBAR() asm volatile("s_waitcnt lgkmcnt(0)\n\ts_barrier" ::: "memory")
#define SB() __builtin_amdgcn_sched_barrier(0)
DI u32x4 ld128(u32 a) {
  return *(const LAS u32x4*)((LAS char*)smem + a); }
DI u32x2 ld64(u32 a) { return *(const LAS u32x2*)((LAS char*)smem + a); }
DI u32 ld32(u32 a) { return *(const LAS u32*)((LAS char*)smem + a); }
DI void st128(u32 a, u32x4 v) { *(LAS u32x4*)((LAS char*)smem + a) = v; }
DI void st64(u32 a, u32x2 v) { *(LAS u32x2*)((LAS char*)smem + a) = v; }
DI void st32(u32 a, u32 v) { *(LAS u32*)((LAS char*)smem + a) = v; }
DI void st16(u32 a, u16 v) { *(LAS u16*)((LAS char*)smem + a) = v; }
DI u32x2 tr64(u32 a) {
  return __builtin_bit_cast(u32x2, __builtin_amdgcn_ds_read_tr16_b64_v4i16((LAS s16x4*)((LAS char*)smem + a))); }
DI u32x4 tr2(u32 a, u32 b) { u32x2 l = tr64(a), h = tr64(b); return u32x4{l[0], l[1], h[0], h[1]}; }
DI f32x4 mma(u32x4 a, u32x4 b, f32x4 c) {
  return __builtin_amdgcn_mfma_f32_16x16x32_bf16(__builtin_bit_cast(bf16x8, a), __builtin_bit_cast(bf16x8, b), c, 0, 0, 0);
}
DI f32x4 mma16(u32x2 a, u32x2 b, f32x4 c) {
  return __builtin_amdgcn_mfma_f32_16x16x16bf16_1k(__builtin_bit_cast(s16x4, a), __builtin_bit_cast(s16x4, b), c, 0, 0, 0);
}
DI u32 pk(float a, float b) { f32x2 v = {a, b}; return __builtin_bit_cast(u32, __builtin_convertvector(v, bf16x2)); }
DI float bf2f(u32 x) { return __uint_as_float(x << 16); }
DI float fmax2(float a, float b) { float r; asm("v_max_f32 %0, %1, %2" : "=v"(r) : "v"(a), "v"(b)); return r; }
DI u32 f2u(float x) { return __float_as_uint(x); }
DI float u2f(u32 x) { return __uint_as_float(x); }

DI float vmax3(float a, float b, float c) { float r; asm("v_max3_f32 %0, %1, %2, %3" : "=v"(r) : "v"(a), "v"(b), "v"(c)); return r; }
DI float vnmax3(float a, float b, float c) { float r; asm("v_max3_f32 %0, -%1, -%2, -%3" : "=v"(r) : "v"(a), "v"(b), "v"(c)); return r; }
DI float vmax3n(float a, float b, float c) { float r; asm("v_max3_f32 %0, %1, -%2, -%3" : "=v"(r) : "v"(a), "v"(b), "v"(c)); return r; }
template <bool MAX> DI void row_reduce(f32x2& a, f32x2& b) {
  auto r = __builtin_amdgcn_permlane16_swap(f2u(a[0]), f2u(a[1]), false, false);
  auto s = __builtin_amdgcn_permlane16_swap(f2u(b[0]), f2u(b[1]), false, false);
  float ta, tb;
  if (MAX) asm("v_max_f32 %0, %2, %3\n\tv_max_f32 %1, %4, %5\n\ts_nop 1" : "=&v"(ta), "=v"(tb) : "v"(r[0]), "v"(r[1]), "v"(s[0]), "v"(s[1]));
  else { ta = u2f(r[0]) + u2f(r[1]); tb = u2f(s[0]) + u2f(s[1]); }
  auto t = __builtin_amdgcn_permlane32_swap(f2u(ta), f2u(tb), false, false);
  const float z = MAX ? fmax2(u2f(t[0]), u2f(t[1])) : u2f(t[0]) + u2f(t[1]);
  auto v = __builtin_amdgcn_permlane32_swap(f2u(z), f2u(z), false, false);
  auto x = __builtin_amdgcn_permlane16_swap(v[0], v[0], false, false);
  auto y = __builtin_amdgcn_permlane16_swap(v[1], v[1], false, false);
  a = f32x2{u2f(x[0]), u2f(x[1])}; b = f32x2{u2f(y[0]), u2f(y[1])};
}
template <int N, class H>
DI void dual_exp(const f32x4* t0, const f32x4* t1, f32x2 (&e)[N][5], f32x2 (&sm)[N], H& hook, int h0) {
  const float L = 1.44269504088896340736f;
  const f32x2 LL = {L, -L};
  f32x2 mx[N];
#pragma unroll
  for (int i = 0; i < N; ++i)
    mx[i] = f32x2{vmax3(vmax3(t0[i][0], t0[i][1], t0[i][2]), t0[i][3], t1[i][0]), vmax3n(vnmax3(t0[i][0], t0[i][1], t0[i][2]), t0[i][3], t1[i][0])};
  hook(h0);
#pragma unroll
  for (int i = 0; i < N; i += 2) row_reduce<true>(mx[i], mx[i + 1]);
#pragma unroll
  for (int i = 0; i < N; ++i) {
    hook(h0 + 1 + i);
    const f32x2 m = mx[i] * -L;
#pragma unroll
    for (int k = 0; k < 5; ++k) {
      const float p = k < 4 ? t0[i][k & 3] : t1[i][0];
      const f32x2 a = __builtin_elementwise_fma(f32x2{p, p}, LL, m);
      e[i][k] = f32x2{__builtin_amdgcn_exp2f(a[0]), __builtin_amdgcn_exp2f(a[1])};
    }
  }
  hook(h0 + 1 + N);
#pragma unroll
  for (int i = 0; i < N; ++i) sm[i] = ((e[i][0] + e[i][1]) + (e[i][2] + e[i][3])) + e[i][4];
#pragma unroll
  for (int i = 0; i < N; i += 2) row_reduce<false>(sm[i], sm[i + 1]);
}
DI u32x4 wphi_frag(int h, int ft, int off, int g, int q, int p) {
  const int d0 = 4 * g - off, d1 = d0 + 16;
  const u32 b = L_WPHI + h * 1280 + 32 * ft + 8 * p + q * 64;
  return tr2(d0 >= 0 ? b + d0 * 64 : L_ZERO, d1 <= 16 ? b + d1 * 64 : L_ZERO);
}
DI bool spin_more(u32& spins) { return ++spins < SPIN_CAP; }
DI int wrap10(int s) { return s >= 10 ? s - 10 : s; }
#define WF_ARG
#define WF_PASS
DI void wait_flags(rsrc_t rw, u32 off, u32 tagA, u32 tagB, int lane WF_ARG) {
  u32 spins = 0;
  for (;;) {
    asm volatile("" ::: "memory");
    u32x2 f = __builtin_amdgcn_raw_buffer_load_b64(rw, off + (lane & 7) * 8, 0, SC1);
    if (__all(f[0] == tagA && f[1] == tagB)) break;
    if (!spin_more(spins)) break;
  }
  asm volatile("" ::: "memory");
}

struct Ctx {
  int rg, cg, w, lane, loc, rot; u32 tagA, tagB; u64 epoch;
  rsrc_t rx, rw[3]; u32 vx, x2, vw[3], sw[3], step;
  DI void piece(int t, int i) const {
    const int s = wrap10(rot + t);
    const u32 b = (t & 3) * STG;
    if (i == 0) dma16(rx, vx, s * 128, b + w * 2048);
    else if (i == 3) dma16(rx, vx, s * 128 + x2, b + w * 2048 + 1024);
    else if (i == 1) dma16(rw[0], vw[0], sw[0] + s * step, b + 16384 + w * 1024);
    else if (i == 4) dma16(rw[1], vw[1], sw[1] + s * step, b + 24576 + w * 1024);
    else dma16m(rw[2], vw[2], sw[2] + s * step, b + 32768 + (w & 3) * 1024, (w - 4) >> 31);
  }
};
DI void pair_sum(f32x4 (&a)[5][2], u32 mine, u32 theirs) {
#pragma unroll
  for (int j = 0; j < 5; ++j) st128(mine + j * 1024, __builtin_bit_cast(u32x4, a[j][1]));
  LBAR();
#pragma unroll
  for (int j = 0; j < 5; ++j) a[j][0] += __builtin_bit_cast(f32x4, ld128(theirs + j * 1024));
}
#define PIN10(x) asm volatile("" : "+v"(x[0][0]), "+v"(x[1][0]), "+v"(x[2][0]), "+v"(x[3][0]), "+v"(x[4][0]), "+v"(x[0][1]), "+v"(x[1][1]), "+v"(x[2][1]), "+v"(x[3][1]), "+v"(x[4][1]))
#define PIN5(x) asm volatile("" : "+v"(x[0]), "+v"(x[1]), "+v"(x[2]), "+v"(x[3]), "+v"(x[4]))
#define ST_ARG
#define ST_PASS
#define POLL_DELAY 12
DI float vfma(float a, float b, float c) { float r; asm("v_fma_f32 %0, %1, %2, %3" : "=v"(r) : "v"(a), "v"(b), "v"(c)); return r; }
DI float vfmac(float c, float a, float b) { asm("v_fmac_f32 %0, %1, %2" : "+v"(c) : "v"(a), "v"(b)); return c; }
DI float vmul(float a, float b) { float r; asm("v_mul_f32 %0, %1, %2" : "=v"(r) : "v"(a), "v"(b)); return r; }
DI void epoch_issue(Ctx& C, const char* p) { asm volatile("s_load_dwordx2 %0, %1, 0x0" : "=s"(C.epoch) : "s"(p)); }
DI void epoch_finish(Ctx& C) {
  asm volatile("s_waitcnt lgkmcnt(0)" : "+s"(C.epoch));
  C.epoch += 1; C.tagA = (u32)C.epoch; C.tagB = (u32)(C.epoch >> 32) + C.tagA * 0x9E3779B1u;
}

#define OUT_AUX 18
#define PV_AT 4
DI void chainA(const u16* __restrict__ Wphi, const u16* __restrict__ Wo, float* __restrict__ out, char* ws, Ctx& C ST_ARG) {
  const int rg = C.rg, cg = C.cg, w = C.w, lane = C.lane, tid = w * 64 + lane, rot = C.rot;
  const int c = lane & 15, g = lane >> 4, q = (lane >> 2) & 3, p = lane & 3, tg = w & 3, u = w >> 2;
  const int m2 = (w - 2) >> 31;
  const rsrc_t rw = C.rw[0], rws = mk(ws, WS_USED), ro = mk(Wo, 640 * 1280);
  epoch_issue(C, ws + WS_EA);
  u32 wv[5];
  {
    const rsrc_t rp = mk(Wphi + cg * 1600, 3200);
#pragma unroll
    for (int i = 0; i < 5; ++i) {
      const u32 idx = tid + 512 * i, h = idx / 640, r = idx - h * 640, d = r >> 5, col = r & 31;
      const u32 off = (h * 400 + d * 20 + (col < 16 ? col : 12 + (col >> 2))) * 2;
      asm volatile("buffer_load_ushort %0, %1, %2, 0 offen" : "=v"(wv[i]) : "v"((col < 16 || (col & 3) == 0) ? off : OOB), "s"(rp));
    }
  }
  u32 vq[2], vo[2];
#pragma unroll
  for (int i = 0; i < 2; ++i) {
    const u32 o = 1024 * ((i ? w & 1 : w) + 8 * i) + 16 * lane, r = o / 160, e = (80 * cg) * 2 + (o - 160 * r);
    vq[i] = (r ^ (((r >> 3) & 1) << 2)) * 3840 + e; vo[i] = r * 1280 + e;
  }
  auto wq_piece = [&](int n) {
    const int t = n >> 1;
    const u32 b = t < 8 ? L_WQ + t * 10240 : (t - 8) * 10240, so = wrap10(rot + t) * 245760;
    if (n & 1) dma16m(rw, vq[1], so, b + 8192 + (w & 1) * 1024, m2); else dma16(rw, vq[0], so, b + w * 1024);
  };
  auto wo_piece = [&](int n) {
    const int t = n >> 1;
    const u32 b = t < 5 ? L_WO0 + t * 10240 : L_WO1 + (t - 5) * 10240;
    if (n & 1) dma16m(ro, vo[1], t * 81920, b + 8192 + (w & 1) * 1024, m2); else dma16(ro, vo[0], t * 81920, b + w * 1024);
  };
  u32 kv_a, kv_b, kv_o, rd_o, fn_o, fpos[2];
  {
    const int j = w >> 1, dt = w & 1;
    kv_a = L_PHI + j * 13312 + (4 * g + q) * 104 + 8 * p;
    kv_b = L_V + (4 * g + q) * 168 + ((dt == 0 || p == j) ? 40 * j + (dt ? 32 : 8 * p) : 160);
    const int e1 = (c >> 2) == j ? 64 + (c & 3) + 4 * g : 80 + g;
    const bool act = dt == 0 || (c >> 2) == j || c == 4 * ((j + 1) & 3);
    kv_o = act ? WS_KVP + (u32)((cg * 16 + rg) * 1008 + j * 252 + (dt == 0 ? lane : e1)) * 16 : OOB;
    rd_o = tid < 252 ? WS_KVP + (u32)((cg * 16 + 4 * (tid & 3)) * 1008 + 63 * rg + (tid >> 2)) * 16 : OOB;
    fn_o = (tid < 252 && (tid & 3) == 0) ? WS_KVF + (u32)(cg * 1008 + 63 * rg + (tid >> 2)) * 16 : OOB;
#pragma unroll
    for (int i = 0; i < 2; ++i) {
      const u32 ga = tid + 512 * i, h = ga / 252, r = ga - 252 * h, i3 = r / 84, e = r - 84 * i3;
      fpos[i] = ga < 1008 ? L_KVI + h * 2016 + (i3 < 2 ? e * 16 + 8 * i3 : 1344 + e * 8) : L_SINKA;
    }
    asm volatile("" : "+v"(kv_a), "+v"(kv_b), "+v"(kv_o), "+v"(rd_o), "+v"(fn_o), "+v"(fpos[0]), "+v"(fpos[1]));
  }
#pragma unroll
  for (int i = 0; i < 5; ++i) C.piece(1, i);
  STAMP(1);
  epoch_finish(C);
  if (tid < 16) st32(L_ZERO + tid * 4, 0);
  u32x4 wf[4][2];

  f32x4 acc[5][2];
  u32x4 xo[10][2];
#pragma unroll
  for (int j = 0; j < 5; ++j) acc[j][0] = acc[j][1] = f32x4{0, 0, 0, 0};
  {
    const u32 xs = ((32 * tg + c) * 128 + ((g ^ (c & 7)) << 4)) ^ (u * 64), xb0 = xs + u * 2048, xb1 = xs + (1 - u) * 2048;
    const int sb = (g & 1) * (u ? -32 : 32);
    const u32 wr = 16384 + (8 * g + q) * 320 + 8 * p + 160 * u, w1st = wr + u * 10240, wl = wr + (1 - u) * 10240;
    u32x4 fx[2], fw[2][5];
    u32 ba[2][8] = {{xb0, xb1, xb0 ^ 64, xb1 ^ 64, w1st + sb, w1st - sb, wl + sb, wl - sb}, {}};
#pragma unroll
    for (int i = 0; i < 8; ++i) { ba[1][i] = ba[0][i] + 2 * STG; asm volatile("" : "+v"(ba[0][i]), "+v"(ba[1][i])); }
    auto rd = [&](int hs, int j) {
      const int h = hs & 1, hi = (hs >> 2) & 1;
      const u32 b = ((hs >> 1) & 1) * STG, a = ba[hi][4 + 2 * h + (j & 1)] + b + 32 * j;
      if (j < 2) (h ? fx[j] : xo[hs >> 1][j]) = ld128(ba[hi][2 * h + j] + b);
      fw[h][j] = tr2(a, a + 1280);
    };
    auto dm = [&](int hs, int n) {
      if (hs == 0) C.piece(2, n);
      else if (hs <= 14) { const int i = (hs & 1) ? n : n + 3; if (n < 3 && i < 5) C.piece((hs + 5) >> 1, i); }
      else if (hs == 15 && n == 0) touch(ro, lane < 25 ? (u32)(C.loc * 200 + 25 * w + lane) * 128 : OOB, L_SINKA);
      else if (n == 2) wq_piece(hs - 15);
    };
    WAITV(5);
#pragma unroll
    for (int i = 0; i < 5; ++i) { asm volatile("" : "+v"(wv[i])); st16(L_WPHI + (tid + 512 * i) * 2, (u16)wv[i]); }
    BAR();
    STAMP(2);
#pragma unroll
    for (int j = 0; j < 5; ++j) rd(0, j);
#pragma unroll
    for (int hs = 0; hs < 20; ++hs) {
      const int h = hs & 1;
      if (h == 1 && hs < 19) {
        if (hs < 17) WAITV(5); else WAITV(3);
        BAR();
        STAMP(20 + (hs >> 1));
      }
      const u32x4 x0 = h ? fx[0] : xo[hs >> 1][0], x1 = h ? fx[1] : xo[hs >> 1][1];
      SB();
#pragma unroll
      for (int j = 0; j < 5; ++j) {
        acc[j][0] = mma(fw[h][j], x0, acc[j][0]);
        SB();
        if (hs < 19) rd(hs + 1, j);
        SB();
        acc[j][1] = mma(fw[h][j], x1, acc[j][1]);
        SB();
        dm(hs, j);
        if (hs == 16 && j < 4) wf[j >> 1][j & 1] = wphi_frag(2 * u + (j >> 1), j & 1, 4 * (j >> 1), g, q, p);
        SB();
      }
    }
  }
  STAMP(3);
  {
    u32x2 kp[3][2];
#pragma unroll
    for (int j = 0; j < 3; ++j)
#pragma unroll
      for (int tt = 0; tt < 2; ++tt) kp[j][tt] = u32x2{pk(acc[j][tt][0], acc[j][tt][1]), pk(acc[j][tt][2], acc[j][tt][3])};
    f32x4 t0[4], t1[4];
#pragma unroll
    for (int hh = 0; hh < 2; ++hh)
#pragma unroll
      for (int tt = 0; tt < 2; ++tt) {
        const u32x4 b = {kp[hh][tt][0], kp[hh][tt][1], kp[hh + 1][tt][0], kp[hh + 1][tt][1]};
        t0[2 * hh + tt] = mma(wf[hh][0], b, f32x4{0, 0, 0, 0}); t1[2 * hh + tt] = mma(wf[hh][1], b, f32x4{0, 0, 0, 0});
      }
    SB();
    const u32 pl = c * 104 + 8 * g, vl = c * 168 + 8 * g, v2l = c * 168 + (g < 2 ? 160 : 80 * u + 8 * g - 16);
    const u32 pa[2] = {pl + (L_PHI + 2 * u * 13312 + (32 * tg + 16 * u) * 104), pl + (L_PHI + 2 * u * 13312 + (32 * tg + 16 * (1 - u)) * 104)};
    auto hook = [&](int i) {
      SB();
      wq_piece(5 + i);
      if (i < 6) {
        const int tt = i / 3, j = 2 + i % 3;
        const u32 rb = L_V + (32 * tg + 16 * (tt ? 1 - u : u)) * 168;
        if (j == 2) st64(v2l + rb, g < 2 ? u32x2{0x3f803f80u, 0x3f803f80u} : kp[2][tt]);
        else st64(vl + (rb + 80 * u + 32 * j - 80), u32x2{pk(acc[j][tt][0], acc[j][tt][1]), pk(acc[j][tt][2], acc[j][tt][3])});
      }
      SB();
    };
    BAR();
    hook(0);
    asm volatile("s_nop 3");
    SB();
    f32x2 e[4][5], sm[4];
    dual_exp<4>(t0, t1, e, sm, hook, 1);
#pragma unroll
    for (int i = 0; i < 4; ++i) {
      hook(7 + i);
      const f32x2 r = {__builtin_amdgcn_rcpf(sm[i][0]), __builtin_amdgcn_rcpf(sm[i][1])};
#pragma unroll
      for (int k = 0; k < 5; ++k) e[i][k] *= r;
      const u32 a = pa[i & 1] + (i >> 1) * 13312;
      st64(a, u32x2{pk(e[i][0][0], e[i][1][0]), pk(e[i][2][0], e[i][3][0])});
      st64(a + 32, u32x2{pk(e[i][0][1], e[i][1][1]), pk(e[i][2][1], e[i][3][1])});
      st64(a + 64, u32x2{pk(e[i][4][0], e[i][4][1]), 0u});
    }
  }
  {
    const u32 ab = kv_a, bb = kv_b, off = kv_o;
    LBAR();
    STAMP(4);
    u32x4 fa[4][3], fb[4];
#pragma unroll
    for (int ks = 0; ks < 4; ++ks) {
      fb[ks] = tr2(bb + ks * 5376, bb + ks * 5376 + 2688);
#pragma unroll
      for (int ft = 0; ft < 3; ++ft) fa[ks][ft] = tr2(ab + ks * 3328 + 32 * ft, ab + ks * 3328 + 32 * ft + 1664);
    }
    SB();
    f32x4 kv[3] = {{0, 0, 0, 0}, {0, 0, 0, 0}, {0, 0, 0, 0}};
#pragma unroll
    for (int ks = 0; ks < 4; ++ks)
#pragma unroll
      for (int ft = 0; ft < 3; ++ft) kv[ft] = mma(fa[ks][ft], fb[ks], kv[ft]);
#pragma unroll
    for (int i = 0; i < 3; ++i)
      __builtin_amdgcn_raw_buffer_store_b128(u32x4{pk(kv[i][0], kv[i][1]), C.tagA, pk(kv[i][2], kv[i][3]), C.tagB}, rws, off + i * 84 * 16, 0, SC1);
  }
  STAMP(5);
  f32x4 qa[5][2];
#pragma unroll
  for (int j = 0; j < 5; ++j) qa[j][0] = qa[j][1] = f32x4{0, 0, 0, 0};
  {
    const int r1 = 8 * g + 4 * (g & 1) + q;
    const u32 w1 = r1 * 160 + 8 * p + u * 5120, w2 = (r1 ^ 4) * 160 + 8 * p + u * 5120;
    u32x4 fw[2][5], pv[4];
    u32 qb[3][2] = {{w1, w2}, {w1 + L_WQ, w2 + L_WQ}, {w1 + L_WQ + 61440, w2 + L_WQ + 61440}};
#pragma unroll
    for (int i = 0; i < 3; ++i) asm volatile("" : "+v"(qb[i][0]), "+v"(qb[i][1]));
    auto rd = [&](int t, int j) {
      const int set = t < 6 ? 1 : (t < 8 ? 2 : 0);
      const u32 o = (t - (t < 6 ? 0 : (t < 8 ? 6 : 8))) * 10240 + 32 * j;
      fw[t & 1][j] = tr2(qb[set][0] + o, qb[set][1] + o);
    };
    const u32 roff = rd_o, fo = fn_o;
    WAITV(3);
    BAR();
    STAMP(31);
#pragma unroll
    for (int j = 0; j < 5; ++j) rd(0, j);
#pragma unroll
    for (int t = 0; t < 10; ++t) {
      if (t == 7) {
        PIN10(qa);
        STAMP(30);
        asm volatile("s_waitcnt vmcnt(%4)" : "+v"(pv[0]), "+v"(pv[1]), "+v"(pv[2]), "+v"(pv[3]) : "i"(PV_AT < 5 ? 14 - 3 * PV_AT : 0) : "memory");
        u32 spins = 0;
        for (;;) {
          bool ok = true;
#pragma unroll
          for (int i = 0; i < 4; ++i) ok &= pv[i][1] == C.tagA && pv[i][3] == C.tagB;
          if (spins == 0) STAMP(33);
          if (spins == 1) STAMP(34);
          if (__all(ok || tid >= 252)) break;
          if (!spin_more(spins)) break;
          asm volatile("" ::: "memory");
#pragma unroll
          for (int i = 0; i < 4; ++i) pv[i] = __builtin_amdgcn_raw_buffer_load_b128(rws, roff, i * 1008 * 16, SC1);
        }
        float sm[4];
#pragma unroll
        for (int k = 0; k < 4; ++k) {
          float v[4];
#pragma unroll
          for (int i = 0; i < 4; ++i) v[i] = (k & 1) ? u2f(pv[i][k & 2] & 0xffff0000u) : bf2f(pv[i][k & 2]);
          sm[k] = (v[0] + v[1]) + (v[2] + v[3]);
          sm[k] += u2f(__builtin_amdgcn_mov_dpp(f2u(sm[k]), 0xB1, 0xf, 0xf, true));
          sm[k] += u2f(__builtin_amdgcn_mov_dpp(f2u(sm[k]), 0x4E, 0xf, 0xf, true));
        }
        __builtin_amdgcn_raw_buffer_store_b128(u32x4{pk(sm[0], sm[1]), C.tagA, pk(sm[2], sm[3]), C.tagB}, rws, fo, 0, SC1);
        STAMP(6);
        BAR();
      }
      SB();
#pragma unroll
      for (int j = 0; j < 5; ++j) {
        qa[j][0] = mma(fw[t & 1][j], xo[t][0], qa[j][0]);
        SB();
        if (t < 9) rd(t + 1, j);
        SB();
        qa[j][1] = mma(fw[t & 1][j], xo[t][1], qa[j][1]);
        SB();
        if (t == PV_AT && j == 0) {
#pragma unroll
          for (int i = 0; i < 4; ++i) asm volatile("buffer_load_dwordx4 %0, %1, %2, %3 offen sc1" : "=v"(pv[i]) : "v"(roff), "s"(rws), "s"(i * 1008 * 16));
        }
        if ((j & 1) == 0) { const int n = 3 * t + (j >> 1); if (n < 4) wq_piece(16 + n); else if (n < 14) wo_piece(n - 4); }
        if (t >= 8 && j < 4) wf[j][t - 8] = wphi_frag(j, t - 8, 4 * j, g, q, p);
        SB();
      }
    }
    pair_sum(qa, L_WQ + w * 5120 + lane * 16, L_WQ + (w ^ 4) * 5120 + lane * 16);
  }
  STAMP(7);
  u32x4 pq0[4], fv[2]; u32 pq1[4]; float eps[4];
  const u32 fa0 = WS_KVF + (u32)(cg * 1008 + tid) * 16, fa1 = tid < 496 ? fa0 + 8192 : OOB;
  {
    u32x2 qp[5];
#pragma unroll
    for (int j = 0; j < 5; ++j) qp[j] = u32x2{pk(qa[j][0][0], qa[j][0][1]), pk(qa[j][0][2], qa[j][0][3])};
    f32x4 t0[4], t1[4];
#pragma unroll
    for (int j = 0; j < 4; ++j) {
      const u32x4 b = {qp[j][0], qp[j][1], qp[j + 1][0], qp[j + 1][1]};
      t0[j] = mma(wf[j][0], b, f32x4{0, 0, 0, 0}); t1[j] = mma(wf[j][1], b, f32x4{0, 0, 0, 0});
    }
    SB();
    LBAR();
    SB();
    asm volatile("s_nop 7");
    SB();
    f32x2 e[4][5], sm[4];
    auto hook = [&](int i) {
      SB();
      if (i < 5) { wo_piece(10 + 2 * i); wo_piece(11 + 2 * i); }
      else {
        asm volatile("buffer_load_dwordx4 %0, %1, %2, 0 offen sc1" : "=v"(fv[0]) : "v"(fa0), "s"(rws));
        asm volatile("buffer_load_dwordx4 %0, %1, %2, 0 offen sc1" : "=v"(fv[1]) : "v"(fa1), "s"(rws));
      }
      SB();
    };
    dual_exp<4>(t0, t1, e, sm, hook, 0);
#pragma unroll
    for (int j = 0; j < 4; ++j) {
      const float r = sm[j][0] * __builtin_amdgcn_rcpf(sm[j][1]);
      pq0[j] = u32x4{pk(e[j][0][0], e[j][1][0]), pk(e[j][2][0], e[j][3][0]), pk(e[j][0][1] * r, e[j][1][1] * r), pk(e[j][2][1] * r, e[j][3][1] * r)};
      pq1[j] = pk(e[j][4][0], e[j][4][1] * r);
      eps[j] = 1e-6f * sm[j][0];
    }
  }
  asm volatile("" : "+v"(pq0[0]), "+v"(pq0[1]), "+v"(pq0[2]), "+v"(pq0[3]), "+v"(pq1[0]), "+v"(pq1[1]), "+v"(pq1[2]), "+v"(pq1[3]), "+v"(eps[0]),
               "+v"(eps[1]), "+v"(eps[2]), "+v"(eps[3]));
  STAMP(8);
  {
    asm volatile("s_waitcnt vmcnt(0)" : "+v"(fv[0]), "+v"(fv[1]) :: "memory");
    u32 spins = 0;
    for (;;) {
      if (spins == 1) STAMP(35);
      if (__all(fv[0][1] == C.tagA && fv[0][3] == C.tagB && ((fv[1][1] == C.tagA && fv[1][3] == C.tagB) || tid >= 496))) break;
      if (!spin_more(spins)) break;
      asm volatile("" ::: "memory");
      fv[0] = __builtin_amdgcn_raw_buffer_load_b128(rws, fa0, 0, SC1);
      fv[1] = __builtin_amdgcn_raw_buffer_load_b128(rws, fa1, 0, SC1);
    }
#pragma unroll
    for (int i = 0; i < 2; ++i) st64(fpos[i], u32x2{fv[i][0], fv[i][2]});
    LBAR();
  }
  STAMP(9);
  const int tile = 2 * tg + u;
  u32x4 f0[8], f1[8]; u32x2 fe[8];
  {
    u32x2 oq[4], ex = {0u, 0u};
    f32x4 t0[4], t1[4];
#pragma unroll
    for (int j = 0; j < 4; ++j) {
      const int e1 = (c >> 2) == j ? 64 + (c & 3) + 4 * g : 80 + g;
      const u32 kb = L_KVI + j * 2016;
      t0[j] = mma(ld128(kb + lane * 16), pq0[j], f32x4{0, 0, 0, 0});
      t1[j] = mma(ld128(kb + e1 * 16), pq0[j], f32x4{0, 0, 0, 0});
      t0[j] = mma16(u32x2{ld32(kb + 1344 + lane * 8), 0u}, u32x2{pq1[j], 0u}, t0[j]);
      t1[j] = mma16(u32x2{ld32(kb + 1344 + e1 * 8), 0u}, u32x2{pq1[j], 0u}, t1[j]);
    }
#pragma unroll
    for (int j = 0; j < 4; ++j) {
      auto sw = __builtin_amdgcn_permlane16_swap(f2u(t1[j][0]), f2u(t1[j][0]), false, false);
      const float r = __builtin_amdgcn_rcpf(u2f(sw[(j & 1) ^ 1]) + eps[j]);
      oq[j] = u32x2{pk(t0[j][0] * r, t0[j][1] * r), pk(t0[j][2] * r, t0[j][3] * r)};
      const u32x2 e = {pk(t1[j][0] * r, t1[j][1] * r), pk(t1[j][2] * r, t1[j][3] * r)};
      ex = g == j ? e : ex;
    }
    f0[0] = u32x4{oq[0][0], oq[0][1], oq[1][0], oq[1][1]}; f1[0] = u32x4{oq[2][0], oq[2][1], oq[3][0], oq[3][1]}; fe[0] = ex;
    const u32 ob = WS_OFR + (u32)((rg * 8 + tile) * 8 + cg) * 2560;
    __builtin_amdgcn_raw_buffer_store_b128(f0[0], rws, ob + lane * 16, 0, SC1);
    __builtin_amdgcn_raw_buffer_store_b128(f1[0], rws, ob + lane * 16 + 1024, 0, SC1);
    __builtin_amdgcn_raw_buffer_store_b64(ex, rws, ob + lane * 8 + 2048, 0, SC1);
    STAMP(32);
    WAITV(0);
    __builtin_amdgcn_raw_buffer_store_b64(u32x2{C.tagA, C.tagB}, rws, lane == 0 ? WS_OFLAG + (u32)(rg * 8 + tile) * 128 + cg * 8 : OOB, 0, SC1);
  }
  STAMP(10);
  {
    f32x4 a[5];
#pragma unroll
    for (int j = 0; j < 5; ++j) a[j] = f32x4{0, 0, 0, 0};
    const u32 b0 = (4 * g + q) * 160 + 8 * p, bx = (20 * g + 16 + q) * 160 + 8 * p;
    auto wob = [&](int i) -> u32 { const int pc = (cg + i) & 7; return (pc < 4 ? L_WO0 : L_WO1 - 51200) + pc * 12800; };
    u32x4 fw[2][5];
    u32 a0 = 0, a1 = 0;
    auto adr = [&](int st_) {
      const int i = (st_ / 5) * 2, k = st_ % 5;
      a0 = k < 4 ? wob(i + (k >> 1)) + (k & 1) * 6400 + b0 : wob(i) + bx; a1 = k < 4 ? a0 + 3200 : wob(i + 1) + bx;
    };
    const u32 ob = WS_OFR + (u32)(rg * 8 + tile) * 8 * 2560;
    auto ld = [&](int n) {
      const u32 so = ((cg + n) & 7) * 2560;
      f0[n] = __builtin_amdgcn_raw_buffer_load_b128(rws, ob + lane * 16, so, SC1);
      f1[n] = __builtin_amdgcn_raw_buffer_load_b128(rws, ob + lane * 16 + 1024, so, SC1);
      fe[n] = __builtin_amdgcn_raw_buffer_load_b64(rws, ob + lane * 8 + 2048, so, SC1);
    };
    adr(0);
#pragma unroll
    for (int j = 0; j < 5; ++j) fw[0][j] = tr2(a0 + 32 * j, a1 + 32 * j);
#pragma unroll
    for (int s = 0; s < 20; ++s) {
      const int i = (s / 5) * 2, k = s % 5;
      if (s == 2) {
        PIN5(a);
        __builtin_amdgcn_s_sleep(POLL_DELAY);
        wait_flags(rws, WS_OFLAG + (u32)(rg * 8 + tile) * 128, C.tagA, C.tagB, lane WF_PASS);
        ld(1); ld(2); ld(3);
        if (rg == 0 && cg == 0 && w == 0)
          __builtin_amdgcn_raw_buffer_store_b64(u32x2{(u32)C.epoch, (u32)(C.epoch >> 32)}, rws, lane == 0 ? WS_EA : OOB, 0, SC1);
        STAMP(11);
      }
      if (s < 19) adr(s + 1);
      SB();
      const u32x4 b = k == 4 ? u32x4{fe[i][0], fe[i][1], fe[i + 1][0], fe[i + 1][1]} : ((k & 1) ? f1[i + (k >> 1)] : f0[i + (k >> 1)]);
#pragma unroll
      for (int j = 0; j < 5; ++j) {
        a[j] = mma(fw[s & 1][j], b, a[j]);
        SB();
        if (s < 19) fw[(s + 1) & 1][j] = tr2(a0 + 32 * j, a1 + 32 * j);
        SB();
      }
      if (s == 3 || s == 5 || s == 8 || s == 10) ld(4 + (s - 3) * 2 / 5 + (s == 5 || s == 10));
      SB();
    }
    STAMP(12);
    const rsrc_t rout = mk(out, 2048 * 5120);
    const u32 oo = (u32)(128 * rg + 16 * tile + c) * 5120 + (80 * cg + 4 * g) * 4;
#pragma unroll
    for (int j = 0; j < 5; ++j) __builtin_amdgcn_raw_buffer_store_b128(__builtin_bit_cast(u32x4, a[j]), rout, oo + 64 * j, 0, OUT_AUX);
  }
}
#define E_DELAY 16

template <int NT, class FD>
DI void b_gemm(int js0, int tg, int lane, FD& dm, f32x4 (&acc)[3][2][2], f32x4 (&par)[3], float (&nli)[3] ST_ARG) {
  const int c = lane & 15, g = lane >> 4;
#pragma unroll
  for (int j = 0; j < NT; ++j) acc[j][0][0] = acc[j][0][1] = acc[j][1][0] = acc[j][1][1] = f32x4{0, 0, 0, 0};
  {
    const u32 sw = (g ^ (c & 7)) << 4, xb = (32 * tg + c) * 128 + sw, wb = 16384 + (16 * js0 + c) * 128 + sw;
    u32x4 fx[2][2], fw[2][NT][2];
    u32 ba[2][4] = {{xb, xb ^ 64, wb, wb ^ 64}, {}};
#pragma unroll
    for (int i = 0; i < 4; ++i) { ba[1][i] = ba[0][i] + 2 * STG; asm volatile("" : "+v"(ba[0][i]), "+v"(ba[1][i])); }
    auto rd = [&](int hs, int n) {
      const int kk = hs & 1, hi = (hs >> 2) & 1;
      const u32 b = ((hs >> 1) & 1) * STG;
      if (n == 0) { fx[kk][0] = ld128(ba[hi][kk] + b); fx[kk][1] = ld128(ba[hi][kk] + b + 2048); }
      fw[kk][n >> 1][n & 1] = ld128(ba[hi][2 + kk] + b + (n >> 1) * 2048 + (n & 1) * 10240);
    };
    LBAR();
    STAMP(2);
#pragma unroll
    for (int n = 0; n < 2 * NT; ++n) rd(0, n);
#pragma unroll
    for (int hs = 0; hs < 20; ++hs) {
      const int kk = hs & 1;
      if (kk == 1 && hs < 19) { if (hs < 17) WAITV(5); else WAITV(1); BAR(); STAMP(20 + (hs >> 1)); }
      SB();
#pragma unroll
      for (int n = 0; n < 2 * NT; ++n) {
        acc[n >> 1][0][n & 1] = mma(fx[kk][0], fw[kk][n >> 1][n & 1], acc[n >> 1][0][n & 1]);
        SB();
        if (hs < 19) rd(hs + 1, n);
        SB();
        acc[n >> 1][1][n & 1] = mma(fx[kk][1], fw[kk][n >> 1][n & 1], acc[n >> 1][1][n & 1]);
        SB();
        if (n < 4) dm(hs, n);
        if (n == 3) dm(hs, 4);
        SB();
      }
    }
  }
  STAMP(3);
#pragma unroll
  for (int j = 0; j < NT; ++j) { par[j] = __builtin_bit_cast(f32x4, ld128(L_PAR + (16 * (js0 + j) + c) * 32)); nli[j] = -par[j][1]; }
  SB();
  asm volatile("s_nop 7");
  SB();
  {
    float er[NT], ei[NT];
#pragma unroll
    for (int j = 0; j < NT; ++j) { er[j] = acc[j][0][0][0]; ei[j] = acc[j][0][1][0]; }
#pragma unroll
    for (int t = 1; t < 8; ++t)
#pragma unroll
      for (int j = 0; j < NT; ++j) {
        const float nr = vfmac(vfma(nli[j], ei[j], acc[j][t >> 2][0][t & 3]), par[j][0], er[j]);
        ei[j] = vfmac(vfma(par[j][1], er[j], acc[j][t >> 2][1][t & 3]), par[j][0], ei[j]);
        er[j] = nr;
      }
#pragma unroll
    for (int j = 0; j < NT; ++j) st64(L_CAR + ((4 * tg + g) * 80 + 16 * (js0 + j) + c) * 8, u32x2{f2u(er[j]), f2u(ei[j])});
  }
}
template <int NT, class FP>
DI void b_scan(int js0, int tg, int lane, FP& post, f32x4 (&acc)[3][2][2], f32x4 (&par)[3], float (&nli)[3]) {
  const int c = lane & 15, g = lane >> 4;
  {
    float er[NT], ei[NT], yr[NT][8], yi[NT][8], ncr[NT], nci[NT];
#pragma unroll
    for (int j = 0; j < NT; ++j) {
      const u32x2 h0 = ld64(L_CAR + ((4 * tg + g) * 80 + 16 * (js0 + j) + c) * 8);
      er[j] = u2f(h0[0]); ei[j] = u2f(h0[1]); ncr[j] = -par[j][2]; nci[j] = -par[j][3];
    }
#pragma unroll
    for (int j = 0; j < NT; ++j) {
#pragma unroll
      for (int t = 0; t < 8; ++t) {
        if ((t & 1) == 0 && j < 2) { SB(); post(4 * j + (t >> 1)); SB(); }
        const float nr = vfmac(vfmac(acc[j][t >> 2][0][t & 3], nli[j], ei[j]), par[j][0], er[j]);
        ei[j] = vfmac(vfmac(acc[j][t >> 2][1][t & 3], par[j][1], er[j]), par[j][0], ei[j]);
        er[j] = nr;
        yr[j][t] = vfmac(vmul(par[j][2], er[j]), nci[j], ei[j]); yi[j][t] = vfmac(vmul(ncr[j], ei[j]), nci[j], er[j]);
      }
      const u32 hb = L_H + (32 * (js0 + j) + c) * 272 + 64 * tg + 16 * g;
      st128(hb, u32x4{pk(yr[j][0], yr[j][1]), pk(yr[j][2], yr[j][3]), pk(yr[j][4], yr[j][5]), pk(yr[j][6], yr[j][7])});
      st128(hb + 16 * 272, u32x4{pk(yi[j][0], yi[j][1]), pk(yi[j][2], yi[j][3]), pk(yi[j][4], yi[j][5]), pk(yi[j][6], yi[j][7])});
      SB();
    }
  }
}

DI void chainB(const u16* __restrict__ Cre, const u16* __restrict__ Cim, float* __restrict__ out, char* ws, Ctx& C, u32 plr, u32 pli, u32 pdt,
               u32x2 (&dv)[5] ST_ARG) {
  const int rg = C.rg, cg = C.cg, w = C.w, lane = C.lane, tid = w * 64 + lane, rot = C.rot;
  const int c = lane & 15, g = lane >> 4, q = (lane >> 2) & 3, p = lane & 3, tg = w & 3;
  const int m4 = (w - 4) >> 31;
  const rsrc_t rx = C.rx, rws = mk(ws, WS_USED);
  const u32 vw = C.vw[0], r0 = C.sw[0], r1 = C.sw[1], r2 = C.sw[2];
#pragma unroll
  for (int i = 0; i < 5; ++i) C.piece(1, i);
  epoch_issue(C, ws + WS_EB);
  const rsrc_t rc0 = mk(Cre, 640 * 1280), rc1 = mk(w < 2 ? Cre : Cim, 640 * 1280), rc2 = mk(Cim, 640 * 1280);
  auto cslot = [](int j) -> u32 { return (j < 3 ? 4 + j : (j == 3 ? 3 : (j < 7 ? j - 4 : (j == 7 ? 7 : j - 4)))) * 20480; };
  auto c_piece = [&](int j, int i) {
    const u32 b = cslot(j), so = wrap10(rot + j) * 128;
    if (i == 0) dma16(rc0, vw, r0 + so, b + w * 1024);
    if (i == 1) dma16(rc1, vw, r1 + so, b + 8192 + w * 1024);
    if (i == 2) dma16m(rc2, vw, r2 + so, b + 16384 + (w & 3) * 1024, m4);
  };
  auto dm = [&](int hs, int n) {
    if (hs == 0) C.piece(2, n);
    else if (hs <= 14) { const int i = (hs & 1) ? n : n + 3; if (n < 3 && i < 5) C.piece((hs + 5) >> 1, i); }
    else if (hs == 15 && n == 0)
      touch(mk(C.loc < 16 ? Cre : Cim, 640 * 1280), lane < 50 ? (u32)((C.loc & 15) * 400 + 50 * w + lane) * 128 : OOB, L_SINKB);
  };
  STAMP(1);
  epoch_finish(C);
  float l8r = 0.f, l8i = 0.f, lLr = 0.f, lLi = 0.f;
  WAITV(5);
  asm volatile("" : "+v"(plr), "+v"(pli), "+v"(pdt), "+v"(dv[0]), "+v"(dv[1]), "+v"(dv[2]), "+v"(dv[3]), "+v"(dv[4]));
  if (w < 2) {
    const float lr = bf2f(plr), li = bf2f(pli), dt = bf2f(pk(expf(bf2f(pdt)), 0.f) & 0xffffu);
    float sn, cs;
    sincosf(dt * li, &sn, &cs);
    const float e = expf(dt * lr), br = e * cs, bi = e * sn, nr = br - 1.f, dd = 2.f / (lr * lr + li * li);
    float ar = br, ai = bi;
#pragma unroll
    for (int i = 0; i < 7; ++i) {
      const float t = ar * ar - ai * ai; ai = 2.f * ar * ai; ar = t;
      if (i == 2) { l8r = ar; l8i = ai; }
    }
    lLr = ar; lLi = ai;
    if (tid < 80) st128(L_PAR + tid * 32, u32x4{f2u(br), f2u(bi), f2u((nr * lr + bi * li) * dd), f2u((bi * lr - nr * li) * dd)});
  }
  u32x2 xv[5];
  auto ldx = [&]() {
#pragma unroll
    for (int j = 0; j < 5; ++j) xv[j] = __builtin_amdgcn_raw_buffer_load_b64(rx, (16 * w + c) * 1280 + (80 * cg + 4 * g) * 2, 32 * j, 0);
  };
  auto mid = [&]() {
    if (w >= 2) ldx();
    if (w < 2) {
      const int s = tid < 80 ? tid : 0;
      float er[16], ei[16], pr = 0.f, pi = 0.f;
#pragma unroll
      for (int j = 0; j < 16; ++j) { const u32x2 v = ld64(L_CAR + (j * 80 + s) * 8); er[j] = u2f(v[0]); ei[j] = u2f(v[1]); }
#pragma unroll
      for (int j = 0; j < 16; ++j) { const float t = fmaf(l8r, pr, fmaf(-l8i, pi, er[j])); pi = fmaf(l8r, pi, fmaf(l8i, pr, ei[j])); pr = t; }
      const u32 eb = tid < 80 ? WS_EG + (u32)(cg * 16 * 80 + tid) * 16 : OOB;
      __builtin_amdgcn_raw_buffer_store_b128(u32x4{f2u(pr), C.tagA, f2u(pi), C.tagB}, rws, eb + rg * 1280, 0, SC1);
      STAMP(33);
      ldx();
      __builtin_amdgcn_s_sleep(E_DELAY);
      u32x4 ev[15];
      u32 spins = 0;
      for (;;) {
        asm volatile("" ::: "memory");
        bool ok = true;
#pragma unroll
        for (int j = 0; j < 15; ++j) ev[j] = __builtin_amdgcn_raw_buffer_load_b128(rws, j < rg ? eb : OOB, j * 1280, SC1);
#pragma unroll
        for (int j = 0; j < 15; ++j) ok &= (ev[j][1] == C.tagA && ev[j][3] == C.tagB) || j >= rg;
        if (spins == 0) STAMP(34);
        if (spins == 1) STAMP(35);
        if (__all(ok || tid >= 80)) break;
        if (!spin_more(spins)) break;
      }
      STAMP(5);
      float hr = 0.f, hi = 0.f;
#pragma unroll
      for (int j = 0; j < 15; ++j)
        if (j < rg) { const float t = fmaf(lLr, hr, fmaf(-lLi, hi, u2f(ev[j][0]))); hi = fmaf(lLr, hi, fmaf(lLi, hr, u2f(ev[j][2]))); hr = t; }
#pragma unroll
      for (int j = 0; j < 16; ++j) {
        if (tid < 80) st64(L_CAR + (j * 80 + s) * 8, u32x2{f2u(hr), f2u(hi)});
        const float t = fmaf(l8r, hr, fmaf(-l8i, hi, er[j])); hi = fmaf(l8r, hi, fmaf(l8i, hr, ei[j])); hr = t;
      }
    }
  };
  auto post = [&](int t) {
    if (t < 4) c_piece(t, 0), c_piece(t, 2); else c_piece(t - 4, 1);
  };
  {
    f32x4 acc[3][2][2], par[3];
    float nli[3];
    if (w < 4) b_gemm<3>(0, tg, lane, dm, acc, par, nli ST_PASS); else b_gemm<2>(3, tg, lane, dm, acc, par, nli ST_PASS);
    LBAR();
    STAMP(4);
    mid();
    LBAR();
    STAMP(6);
    if (w < 4) b_scan<3>(0, tg, lane, post, acc, par, nli); else b_scan<2>(3, tg, lane, post, acc, par, nli);
    LBAR();
  }
  STAMP(7);
  f32x4 a[5];
  {
    const u32 hb = L_H + (8 * g + q) * 272 + 32 * w + 8 * p;
    const u32 ob = WS_HFR + (u32)((rg * 8 + w) * 40 + 5 * cg) * 1024 + lane * 16;
#pragma unroll
    for (int j = 0; j < 5; ++j) __builtin_amdgcn_raw_buffer_store_b128(tr2(hb + j * 8704, hb + j * 8704 + 1088), rws, ob + j * 1024, 0, SC1);
#pragma unroll
    for (int j = 0; j < 5; ++j) {
      const u32 p0 = pk(bf2f(xv[j][0]) * bf2f(dv[j][0]), u2f(xv[j][0] & 0xffff0000u) * u2f(dv[j][0] & 0xffff0000u));
      const u32 p1 = pk(bf2f(xv[j][1]) * bf2f(dv[j][1]), u2f(xv[j][1] & 0xffff0000u) * u2f(dv[j][1] & 0xffff0000u));
      a[j] = f32x4{bf2f(p0), u2f(p0 & 0xffff0000u), bf2f(p1), u2f(p1 & 0xffff0000u)};
    }
    STAMP(32);
    WAITV(0);
    __builtin_amdgcn_raw_buffer_store_b64(u32x2{C.tagA, C.tagB}, rws, lane == 0 ? WS_HFLAG + (u32)(rg * 8 + w) * 128 + cg * 8 : OOB, 0, SC1);
  }
  STAMP(8);
  BAR();
#pragma unroll
  for (int j = 4; j < 8; ++j)
#pragma unroll
    for (int i = 0; i < 3; ++i) c_piece(j, i);
  wait_flags(rws, WS_HFLAG + (u32)(rg * 8 + w) * 128, C.tagA, C.tagB, lane WF_PASS);
  if (rg == 15 && cg == 0 && w == 0) __builtin_amdgcn_raw_buffer_store_b64(u32x2{(u32)C.epoch, (u32)(C.epoch >> 32)}, rws, lane == 0 ? WS_EB : OOB, 0, SC1);
  STAMP(9);
  {
    u32x4 f[40];
    const u32 ob = WS_HFR + (u32)(rg * 8 + w) * 40 * 1024 + lane * 16;
    auto ld = [&](int i) { f[i] = __builtin_amdgcn_raw_buffer_load_b128(rws, ob + (i & 3) * 1024, wrap10(rot + (i >> 2)) * 4096, SC1); };
    constexpr int PD = 8;
#pragma unroll
    for (int i = 0; i < PD; ++i) ld(i);
    const u32 cb = (g >> 1) * 10240 + c * 128 + (((g & 1) ^ (c & 7)) << 4);
    u32 cbs[3][4];
#pragma unroll
    for (int i = 0; i < 12; ++i) { cbs[i >> 2][i & 3] = (cb ^ (32 * (i & 3))) + (i >> 2) * 61440; asm volatile("" : "+v"(cbs[i >> 2][i & 3])); }
    u32x4 fw[2][5];
    auto rd = [&](int s, int j) { const u32 o = cslot(s >> 2); fw[s & 1][j] = ld128(cbs[o / 61440][s & 3] + o % 61440 + j * 2048); };
#pragma unroll
    for (int j = 0; j < 5; ++j) rd(0, j);
#pragma unroll
    for (int s = 0; s < 40; ++s) {
      if (s == 8) BAR();
      if (s == 31) { WAITV(8); BAR(); }
      SB();
#pragma unroll
      for (int j = 0; j < 5; ++j) {
        a[j] = mma(fw[s & 1][j], f[s], a[j]);
        SB();
        if (s < 39) rd(s + 1, j);
        SB();
      }
      if (s + PD < 40) ld(s + PD);
      if (s >= 8 && s < 14) c_piece(8 + (s - 8) / 3, (s - 8) % 3);
      SB();
    }
    STAMP(10);
    const rsrc_t rout = mk(out, 2048 * 5120);
    const u32 oo = (u32)(128 * rg + 16 * w + c) * 5120 + (640 + 80 * cg + 4 * g) * 4;
#pragma unroll
    for (int j = 0; j < 5; ++j) __builtin_amdgcn_raw_buffer_store_b128(__builtin_bit_cast(u32x4, a[j]), rout, oo + 64 * j, 0, OUT_AUX);
  }
}

__global__ __launch_bounds__(512, 2) void fused_kernel(const u16* Ax, const u16* Wqkv, const u16* Wphi, const u16* Wo, const u16* Bx,
                                                       const u16* Lre, const u16* Lim, const u16* Bre, const u16* Bim, const u16* Cre,
                                                       const u16* Cim, const u16* Ldt, const u16* Dsk, float* out, char* ws) {
  const int bid = blockIdx.x, xcd = bid & 7, local = ((xcd & 3) << 5) | (bid >> 3);
  Ctx C;
  C.rg = local >> 3; C.cg = local & 7; C.loc = bid >> 3;
  C.w = __builtin_amdgcn_readfirstlane(threadIdx.x >> 6); C.lane = threadIdx.x & 63;
  C.rot = wrap10(C.cg + (C.cg >> 2) + ((5 * (C.rg & 3) + 1) >> 1));
  asm volatile("" ::"s"(Ax), "s"(Wqkv), "s"(Wphi), "s"(Wo), "s"(Bx), "s"(Lre), "s"(Lim), "s"(Bre), "s"(Bim), "s"(Cre), "s"(Cim), "s"(Ldt), "s"(Dsk), "s"(out), "s"(ws));
  int isA = xcd < 4;
  u32 plr = 0, pli = 0, pdt = 0;
  u32x2 dv[5] = {};
  {
    const int w = C.w, lane = C.lane, cg = C.cg, l8 = lane >> 3;
    const u32 sw = ((lane & 7) ^ l8) << 4;
    C.rx = mk((isA ? Ax : Bx) + C.rg * 128 * 640, 128 * 1280);
    C.vx = (isA ? 16 * w + l8 : 32 * (w >> 1) + 8 * (l8 >> 2) + 4 * (w & 1) + (l8 & 3)) * 1280 + sw;
    C.x2 = isA ? 10240 : 20480;
    asm volatile("s_nop 4");
    C.piece(0, 0); C.piece(0, 3);
    if (isA) {
#pragma unroll
      for (int i = 0; i < 3; ++i) {
        const u32 o = 1024 * ((i < 2 ? w : w & 3) + 8 * i) + 16 * lane, k = o / 320, cp = (o - 320 * k) >> 4;
        const u32 tj = (cp >> 1) ^ ((k >> 3) & 1), uu = tj >= 5, qq = 2 * (tj - 5 * uu) + (cp & 1);
        C.vw[i] = k * 3840 + (640 + 80 * cg + 40 * uu + (qq < 5 ? 8 * qq : 600 + 8 * qq)) * 2;
        C.rw[i] = mk(Wqkv, 640 * 3840); C.sw[i] = 0;
      }
      C.step = 245760;
    } else {
      const u32 off = threadIdx.x < 80 ? (u32)(80 * cg + threadIdx.x) * 2 : OOB;
      asm volatile("buffer_load_ushort %0, %1, %2, 0 offen" : "=v"(plr) : "v"(off), "s"(mk(Lre, 1280)));
      asm volatile("buffer_load_ushort %0, %1, %2, 0 offen" : "=v"(pli) : "v"(off), "s"(mk(Lim, 1280)));
      asm volatile("buffer_load_ushort %0, %1, %2, 0 offen" : "=v"(pdt) : "v"(off), "s"(mk(Ldt, 1280)));
#pragma unroll
      for (int j = 0; j < 5; ++j)
        asm volatile("buffer_load_dwordx2 %0, %1, %2, 0 offen offset:%3" : "=v"(dv[j]) : "v"((80 * cg + 4 * (lane >> 4)) * 2), "s"(mk(Dsk, 1280)), "i"(32 * j));
      C.vw[0] = C.vw[1] = C.vw[2] = l8 * 1280 + sw;
      C.sw[0] = (80 * cg + 8 * w) * 1280; C.sw[1] = w < 2 ? C.sw[0] + 64 * 1280 : C.sw[0] - 16 * 1280; C.sw[2] = (80 * cg + 8 * (w & 3) + 48) * 1280;
      C.rw[0] = mk(Bre, 640 * 1280); C.rw[1] = mk(w < 2 ? Bre : Bim, 640 * 1280); C.rw[2] = mk(Bim, 640 * 1280);
      C.step = 128;
    }
    C.piece(0, 1); C.piece(0, 2); C.piece(0, 4);
    asm volatile("" : "+s"(isA));
  }
  if (isA) chainA(Wphi, Wo, out, ws, C); else chainB(Cre, Cim, out, ws, C, plr, pli, pdt, dv);
}

extern "C" void kernel_launch(void* const* d_in, const int* in_sizes, int n_in, void* d_out, int out_size, void* d_ws, size_t ws_size,
                              hipStream_t stream) {
  fused_kernel<<<256, 512, 0, stream>>>((const u16*)d_in[0], (const u16*)d_in[1], (const u16*)d_in[2], (const u16*)d_in[3], (const u16*)d_in[4],
                                        (const u16*)d_in[5], (const u16*)d_in[6], (const u16*)d_in[7], (const u16*)d_in[8], (const u16*)d_in[9],
                                        (const u16*)d_in[10], (const u16*)d_in[11], (const u16*)d_in[12], (float*)d_out, (char*)d_ws);
}
```
